```python
import torch

def _softmax(x, axis=-1):
    return torch.softmax(x, dim=axis)
def _gelu(x):
    return torch.nn.functional.gelu(x, approximate="tanh")
def _silu(x):
    return torch.nn.functional.silu(x)
def _sigmoid(x):
    return torch.sigmoid(x)
def _layernorm(x, axis=-1, eps=1e-5):
    m = x.mean(dim=axis, keepdim=True)
    v = x.var(dim=axis, keepdim=True, unbiased=False)
    return (x - m) / torch.sqrt(v + eps)
def _scatter_add(tgt, idx, src, axis=0):
    return tgt.clone().index_add_(axis, idx, src)
def _segment_sum(src, seg, n):
    out = torch.zeros((n,) + src.shape[1:], dtype=src.dtype)
    return out.index_add_(0, seg, src)
def _pad(x, pw):
    flat = tuple(v for pair in reversed(pw) for v in pair)
    return torch.nn.functional.pad(x, flat)

kernel_name = 'sequential_preLN__sliding_local_dilated__mixer_channel_mlp__prompt'

def setup_inputs():

    _w = (lambda a: (lambda t: t.to(torch.bfloat16) if t.is_floating_point() else t)(torch.as_tensor(a)))
    def _A_setup():
        B, S, D = 1, 2048, 512
        return {
            "x": _w((lambda *s: torch.randn(*s, dtype=torch.bfloat16) * 0.05)(B, S, D)),
            "wqkv": _w((lambda *s: torch.randn(*s, dtype=torch.bfloat16) * 0.05)(D, 3 * D)),
            "wo": _w((lambda *s: torch.randn(*s, dtype=torch.bfloat16) * 0.05)(D, D)),
        }
    _A_in = _A_setup()

    def _B_setup():
        B, S, D, F = 1, 2048, 512, 1792
        return {
            "x": _w((lambda *s: torch.randn(*s, dtype=torch.bfloat16) * 0.05)(B, S, D)),
            "w1": _w((lambda *s: torch.randn(*s, dtype=torch.bfloat16) * 3.2)(D, F) * (D ** -0.5)),
            "b1": _w((lambda *s: torch.randn(*s, dtype=torch.bfloat16) * 1.6)(F)),
            "w2": _w((lambda *s: torch.randn(*s, dtype=torch.bfloat16) * 0.2)(F, D) * (F ** -0.5)),
            "b2": _w((lambda *s: torch.randn(*s, dtype=torch.bfloat16) * 0.2)(D)),
        }
    _B_in = _B_setup()

    return {
        "A_x": _A_in["x"],
        "A_wqkv": _A_in["wqkv"],
        "A_wo": _A_in["wo"],
        "B_x": _B_in["x"],
        "B_w1": _B_in["w1"],
        "B_b1": _B_in["b1"],
        "B_w2": _B_in["w2"],
        "B_b2": _B_in["b2"],
    }

def reference(A_x, A_wqkv, A_wo, B_x, B_w1, B_b1, B_w2, B_b2):
    def _A_ref(x, wqkv, wo):
        B, S, D = x.shape
        H = 32; Dh = D // H
        W, dil = 8, 2
        qkv = (lambda x, *d: x.permute(*d))((x @ wqkv).reshape(B, S, 3, H, Dh), 2, 0, 3, 1, 4)
        q, k, v = qkv[0], qkv[1], qkv[2]
        scale = Dh ** -0.5
        pos = torch.arange(S)
        off = torch.arange(W) * dil
        idx = pos[:, None] - off[None, :]
        mask = idx >= 0
        idx_c = torch.where(mask, idx, 0).reshape(-1)
        kg = k[:, :, idx_c, :].reshape(B, H, S, W, Dh)
        vg = v[:, :, idx_c, :].reshape(B, H, S, W, Dh)
        logits = torch.einsum("bhsd,bhswd->bhsw", q, kg) * scale
        logits = torch.where(mask[None, None, :, :], logits, -1e9)
        attn = _softmax(logits, axis=-1)
        o = torch.einsum("bhsw,bhswd->bhsd", attn, vg)
        return (lambda x, *d: x.permute(*d))(o, 0, 2, 1, 3).reshape(B, S, D) @ wo

    def _B_ref(x, w1, b1, w2, b2):
        y = _layernorm(x, axis=-1)
        h = _gelu(torch.einsum("bsd,df->bsf", y, w1) + b1)
        return torch.einsum("bsf,fd->bsd", h, w2) + b2

    x = A_x
    h = x + _A_ref(_layernorm(x, axis=-1), A_wqkv, A_wo)
    h2 = h + _B_ref(_layernorm(h, axis=-1), B_w1, B_b1, B_w2, B_b2)
    return h2
```

```cpp
#include <hip/hip_runtime.h>
#include <stdint.h>

typedef __attribute__((ext_vector_type(4))) short s16x4;
typedef __attribute__((ext_vector_type(8))) short s16x8;
typedef __attribute__((ext_vector_type(2))) __bf16 bf16x2;
typedef __attribute__((ext_vector_type(8))) __bf16 bf16x8;
typedef __attribute__((ext_vector_type(4))) float f32x4;
typedef __attribute__((ext_vector_type(4))) unsigned u32x4;
typedef __attribute__((ext_vector_type(2))) unsigned u32x2;
typedef unsigned short u16;
#define LDS_AS __attribute__((address_space(3)))

#define WAIT_V(n) asm volatile("s_waitcnt vmcnt(%0)" ::"n"(n) : "memory")
#define WAIT_L0() asm volatile("s_waitcnt lgkmcnt(0)" ::: "memory")
#define RAW_BARRIER()                  \
  do {                                 \
    WAIT_L0();                         \
    __builtin_amdgcn_s_barrier();      \
    asm volatile("" ::: "memory");     \
  } while (0)

static constexpr int S_ = 2048;
static constexpr int D_ = 512;
static constexpr int F_ = 1792;
static constexpr float EPS = 1e-5f;

__device__ __forceinline__ unsigned pack2(float a, float b) {
  bf16x2 v = {(__bf16)a, (__bf16)b};
  return __builtin_bit_cast(unsigned, v);
}
__device__ __forceinline__ float bflo(unsigned u) { return __uint_as_float(u << 16); }
__device__ __forceinline__ float bfhi(unsigned u) { return __uint_as_float(u & 0xffff0000u); }

__device__ __forceinline__ f32x4 mfma16(bf16x8 a, bf16x8 b, f32x4 c) {
  return __builtin_amdgcn_mfma_f32_16x16x32_bf16(a, b, c, 0, 0, 0);
}
__device__ __forceinline__ bf16x8 lds_a(const char* p) { return *(const bf16x8*)p; }
__device__ __forceinline__ bf16x8 lds_b(const char* p0, const char* p1) {
  s16x4 a = __builtin_amdgcn_ds_read_tr16_b64_v4i16((LDS_AS s16x4*)p0);
  s16x4 b = __builtin_amdgcn_ds_read_tr16_b64_v4i16((LDS_AS s16x4*)p1);
  s16x8 f = __builtin_shufflevector(a, b, 0, 1, 2, 3, 4, 5, 6, 7);
  return __builtin_bit_cast(bf16x8, f);
}
__device__ __forceinline__ float sumsq8(bf16x8 a, float c) {
  bf16x2 p0 = __builtin_shufflevector(a, a, 0, 1);
  bf16x2 p1 = __builtin_shufflevector(a, a, 2, 3);
  bf16x2 p2 = __builtin_shufflevector(a, a, 4, 5);
  bf16x2 p3 = __builtin_shufflevector(a, a, 6, 7);
  c = __builtin_amdgcn_fdot2_f32_bf16(p0, p0, c, false);
  c = __builtin_amdgcn_fdot2_f32_bf16(p1, p1, c, false);
  c = __builtin_amdgcn_fdot2_f32_bf16(p2, p2, c, false);
  c = __builtin_amdgcn_fdot2_f32_bf16(p3, p3, c, false);
  return c;
}
__device__ __forceinline__ float dot2u(unsigned a, unsigned b, float c) {
  return __builtin_amdgcn_fdot2_f32_bf16(__builtin_bit_cast(bf16x2, a), __builtin_bit_cast(bf16x2, b), c, false);
}
__device__ __forceinline__ float dot8(u32x4 a, u32x4 b) {
  const unsigned a0 = a[0], a1 = a[1], a2 = a[2], a3 = a[3];
  const unsigned b0 = b[0], b1 = b[1], b2 = b[2], b3 = b[3];
  float c = 0.f;
  c = dot2u(a0, b0, c);
  c = dot2u(a1, b1, c);
  c = dot2u(a2, b2, c);
  c = dot2u(a3, b3, c);
  return c;
}
__device__ __forceinline__ bf16x8 ones8() {
  s16x8 o = {0x3F80, 0x3F80, 0x3F80, 0x3F80, 0x3F80, 0x3F80, 0x3F80, 0x3F80};
  return __builtin_bit_cast(bf16x8, o);
}
__device__ __forceinline__ void glds16(const void* g, char* l) {
  __builtin_amdgcn_global_load_lds((const unsigned*)g, (unsigned*)l, 16, 0, 0);
}

static constexpr int K1_STAGE = 30 * 1024;
static constexpr int K1_R = 4;
static constexpr int K1_ABYTES = 18 * 1024;
static constexpr int K1_STAT = K1_R * K1_STAGE;
static constexpr int K1_COLP = K1_STAT + 144 * 8;
static constexpr int K1_LDS = K1_COLP + 4 * 96 * 4;
static constexpr int QKV_PITCH = 208;

__device__ __forceinline__ void k1_body(const u16* __restrict__ x, const u16* __restrict__ wqkv, u16* __restrict__ o,
                                        char* smem) {
  const int tid = threadIdx.x, lane = tid & 63;
  const int w = __builtin_amdgcn_readfirstlane(tid >> 6);
  const int rg = w >> 1, ch = w & 1;
  const int bid = blockIdx.x;
  const int panel = (bid & 7) * 2 + (bid >> 7), hp = (bid >> 3) & 15;
  const int row0 = panel * 128 - 16;
  const int g = lane >> 4, l15 = lane & 15, q4 = l15 >> 2, p4 = lane & 3;

  const char* src[4];
  int stride[4];
#pragma unroll
  for (int i = 0; i < 4; ++i) {
    const int p = w + 8 * i;
    if (p < 18) {
      const int rl = 8 * p + (lane >> 3);
      const int chunk = (lane & 7) ^ ((rl >> 1) & 7);
      int gr = row0 + rl;
      gr = gr < 0 ? 0 : gr;
      src[i] = (const char*)(x + (size_t)gr * D_ + chunk * 8);
      stride[i] = 128;
    } else {
      const int pb = (p < 30 ? p : 29) - 18;
      const int b = 4 * pb + g;
      const int c4 = b / 3, pr = b - 3 * c4;
      const int chunk = p4 ^ (2 * ((c4 >> 1) & 1));
      const int krow = 4 * c4 + q4;
      src[i] = (const char*)(wqkv + (size_t)krow * (3 * D_) + pr * D_ + hp * 32 + chunk * 8);
      stride[i] = 64 * 3 * D_ * 2;
    }
  }
  const bool has4 = (w < 6);
  auto issue = [&](int slot) {
    char* base = smem + slot * K1_STAGE + w * 1024;
#pragma unroll
    for (int i = 0; i < 3; ++i) {
      glds16(src[i], base + i * 8192);
      src[i] += stride[i];
    }
    if (has4) {
      glds16(src[3], base + 3 * 8192);
      src[3] += stride[3];
    }
  };

  const int mtA = 1 + 2 * rg + ch, mtB = 2 + 2 * rg - ch;
  const int aoff0 = l15 * 128 + ((g ^ (l15 >> 1)) * 16);
  const int boff = K1_ABYTES + g * 1536 + q4 * 64 + ((ch ^ (g & 1)) * 32) + p4 * 8;

  f32x4 acc[2][3], accH = {0, 0, 0, 0}, accR = {0, 0, 0, 0}, accRH = {0, 0, 0, 0}, accC[3];
#pragma unroll
  for (int j = 0; j < 3; ++j) {
    acc[0][j] = f32x4{0, 0, 0, 0};
    acc[1][j] = f32x4{0, 0, 0, 0};
    accC[j] = f32x4{0, 0, 0, 0};
  }
  float ssq = 0.f, ssqH = 0.f;
  const bf16x8 ones = ones8();

#pragma unroll
  for (int s = 0; s < K1_R - 1; ++s) issue(s);

#pragma unroll
  for (int s = 0; s < 8; ++s) {
    constexpr int dummy = 0;
    (void)dummy;
    const int rem = (7 - s) < (K1_R - 2) ? (7 - s) : (K1_R - 2);
    if (has4) {
      switch (rem) {
        case 0: WAIT_V(0); break;
        case 1: WAIT_V(4); break;
        default: WAIT_V(8); break;
      }
    } else {
      switch (rem) {
        case 0: WAIT_V(0); break;
        case 1: WAIT_V(3); break;
        default: WAIT_V(6); break;
      }
    }
    RAW_BARRIER();
    if (s + K1_R - 1 < 8) issue((s + K1_R - 1) % K1_R);
    const char* st = smem + (s % K1_R) * K1_STAGE;
#pragma unroll
    for (int ks = 0; ks < 2; ++ks) {
      const int ao = aoff0 ^ (ks * 64);
      bf16x8 a0 = lds_a(st + mtA * 2048 + ao);
      bf16x8 a1 = lds_a(st + mtB * 2048 + ao);
      bf16x8 b[3];
#pragma unroll
      for (int j = 0; j < 3; ++j)
        b[j] = lds_b(st + boff + (ks * 8 + 0) * 768 + j * 256, st + boff + (ks * 8 + 1) * 768 + j * 256);
#pragma unroll
      for (int j = 0; j < 3; ++j) {
        acc[0][j] = mfma16(b[j], a0, acc[0][j]);
        acc[1][j] = mfma16(b[j], a1, acc[1][j]);
      }
      accR = mfma16(ones, a0, accR);
      ssq = sumsq8(a0, ssq);
      if (rg < 2) {
        bf16x8 ah = lds_a(st + ao);
        accH = mfma16(rg == 0 ? b[1] : b[2], ah, accH);
        if (w == 0) {
          accRH = mfma16(ones, ah, accRH);
          ssqH = sumsq8(ah, ssqH);
        }
      }
      if (((s * 2 + ks) & 3) == rg) {
#pragma unroll
        for (int j = 0; j < 3; ++j) accC[j] = mfma16(b[j], ones, accC[j]);
      }
    }
  }

  {
    float2* stat = (float2*)(smem + K1_STAT);
    float* colp = (float*)(smem + K1_COLP);
    ssq += __shfl_xor(ssq, 16);
    ssq += __shfl_xor(ssq, 32);
    if (lane < 16) {
      const float mean = accR[0] * (1.f / D_);
      float var = ssq * (1.f / D_) - mean * mean;
      var = var < 0.f ? 0.f : var;
      stat[mtA * 16 + lane] = make_float2(mean, rsqrtf(var + EPS));
    }
    if (w == 0) {
      ssqH += __shfl_xor(ssqH, 16);
      ssqH += __shfl_xor(ssqH, 32);
      if (lane < 16) {
        const float mean = accRH[0] * (1.f / D_);
        float var = ssqH * (1.f / D_) - mean * mean;
        var = var < 0.f ? 0.f : var;
        stat[lane] = make_float2(mean, rsqrtf(var + EPS));
      }
    }
    if (l15 == 0) {
#pragma unroll
      for (int j = 0; j < 3; ++j) *(f32x4*)(colp + rg * 96 + (2 * j + ch) * 16 + 4 * g) = accC[j];
    }
  }
  RAW_BARRIER();
  {
    const float2* stat = (const float2*)(smem + K1_STAT);
    const float* colp = (const float*)(smem + K1_COLP);
    f32x4 cs[3];
#pragma unroll
    for (int j = 0; j < 3; ++j) {
      const int c = (2 * j + ch) * 16 + 4 * g;
      cs[j] = *(const f32x4*)(colp + c) + *(const f32x4*)(colp + 96 + c) + *(const f32x4*)(colp + 192 + c) +
              *(const f32x4*)(colp + 288 + c);
    }
#pragma unroll
    for (int i = 0; i < 2; ++i) {
      const int rl = (i == 0 ? mtA : mtB) * 16 + l15;
      const float2 sv = stat[rl];
#pragma unroll
      for (int j = 0; j < 3; ++j) {
        f32x4 v = (acc[i][j] - sv.x * cs[j]) * sv.y;
        u32x2 pk = {pack2(v[0], v[1]), pack2(v[2], v[3])};
        *(u32x2*)(smem + rl * QKV_PITCH + ((2 * j + ch) * 16 + 4 * g) * 2) = pk;
      }
    }
    if (rg < 2) {
      const float2 sv = stat[l15];
      const int j = 1 + rg;
      f32x4 c = (rg == 0) ? cs[1] : cs[2];
      f32x4 v = (accH - sv.x * c) * sv.y;
      u32x2 pk = {pack2(v[0], v[1]), pack2(v[2], v[3])};
      *(u32x2*)(smem + l15 * QKV_PITCH + ((2 * j + ch) * 16 + 4 * g) * 2) = pk;
    }
  }
  RAW_BARRIER();
  {
    const int half = tid & 1, head = (tid >> 1) & 1, r = tid >> 2;
    const char* base = smem + (r + 16) * QKV_PITCH + (head * 16 + half * 8) * 2;
    const u32x4 qv = *(const u32x4*)base;
    const int grow = panel * 128 + r;
    float lg[8];
#pragma unroll
    for (int wi = 0; wi < 8; ++wi) {
      const u32x4 kv = *(const u32x4*)(base - 2 * wi * QKV_PITCH + 64);
      float d = dot8(qv, kv);
      d += __shfl_xor(d, 1);
      lg[wi] = (grow - 2 * wi >= 0) ? d * 0.25f : -1e9f;
    }
    float mx = lg[0];
#pragma unroll
    for (int wi = 1; wi < 8; ++wi) mx = fmaxf(mx, lg[wi]);
    float sum = 0.f;
#pragma unroll
    for (int wi = 0; wi < 8; ++wi) {
      lg[wi] = __expf(lg[wi] - mx);
      sum += lg[wi];
    }
    const float inv = __builtin_amdgcn_rcpf(sum);
    float ov[8];
#pragma unroll
    for (int d = 0; d < 8; ++d) ov[d] = 0.f;
#pragma unroll
    for (int wi = 0; wi < 8; ++wi) {
      const u32x4 vv = *(const u32x4*)(base - 2 * wi * QKV_PITCH + 128);
      const float p = lg[wi];
#pragma unroll
      for (int d = 0; d < 4; ++d) {
        ov[2 * d] = fmaf(p, bflo(vv[d]), ov[2 * d]);
        ov[2 * d + 1] = fmaf(p, bfhi(vv[d]), ov[2 * d + 1]);
      }
    }
    u32x4 out;
#pragma unroll
    for (int d = 0; d < 4; ++d) out[d] = pack2(ov[2 * d] * inv, ov[2 * d + 1] * inv);
    *(u32x4*)(o + (size_t)grow * D_ + hp * 32 + head * 16 + half * 8) = out;
  }
}

__global__ __launch_bounds__(512, 2) void k1_qkv_attn(const u16* __restrict__ x, const u16* __restrict__ wqkv,
                                                      u16* __restrict__ o) {
  extern __shared__ __attribute__((aligned(1024))) char smem[];
  k1_body(x, wqkv, o, smem);
}

static constexpr int G_STAGE = 16 * 1024;
static constexpr int G_R = 8;
static constexpr int G_LDS = G_R * G_STAGE;

template <int K, bool BIAS>
__device__ __forceinline__ void gemm64_body(const u16* __restrict__ A, const u16* __restrict__ W,
                                            const u16* __restrict__ res, const u16* __restrict__ bias,
                                            u16* __restrict__ out, char* smem) {
  constexpr int NS = K / 64;
  const int tid = threadIdx.x, lane = tid & 63;
  const int w = __builtin_amdgcn_readfirstlane(tid >> 6);
  const int rg = w & 3, ch = w >> 2;
  const int bid = blockIdx.x;
  const int rt = (bid & 7) * 4 + (bid >> 6), ct = (bid >> 3) & 7;
  const int g = lane >> 4, l15 = lane & 15, q4 = l15 >> 2, p4 = lane & 3;

  const char *srcA, *srcB;
  {
    const int rl = 8 * w + (lane >> 3);
    const int chunk = (lane & 7) ^ ((rl >> 1) & 7);
    srcA = (const char*)(A + (size_t)(rt * 64 + rl) * K + chunk * 8);
    const int b = 4 * w + g;
    const int c4 = b >> 1, pr = b & 1;
    const int chunkb = p4 ^ (2 * ((c4 >> 1) & 1));
    const int krow = 4 * c4 + q4;
    srcB = (const char*)(W + (size_t)krow * D_ + ct * 64 + pr * 32 + chunkb * 8);
  }
  auto issue = [&](int slot) {
    char* base = smem + slot * G_STAGE + w * 1024;
    glds16(srcA, base);
    glds16(srcB, base + 8192);
    srcA += 128;
    srcB += 64 * D_ * 2;
  };
  const int aoff0 = rg * 2048 + l15 * 128 + ((g ^ (l15 >> 1)) * 16);
  const int boff = 8192 + g * 1024 + ch * 256 + q4 * 64 + p4 * 8;
  const int sw = (g & 1) * 32;

  f32x4 acc[2] = {f32x4{0, 0, 0, 0}, f32x4{0, 0, 0, 0}};

#pragma unroll
  for (int s = 0; s < G_R - 1; ++s) issue(s);

  auto compute = [&](int slot) {
    const char* st = smem + slot * G_STAGE;
#pragma unroll
    for (int ks = 0; ks < 2; ++ks) {
      bf16x8 a = lds_a(st + (aoff0 ^ (ks * 64)));
#pragma unroll
      for (int j = 0; j < 2; ++j) {
        const int o2 = boff + ((j * 32) ^ sw);
        bf16x8 b = lds_b(st + o2 + (ks * 8 + 0) * 512, st + o2 + (ks * 8 + 1) * 512);
        acc[j] = mfma16(b, a, acc[j]);
      }
    }
  };

  constexpr int NMAIN = NS - (G_R - 1);
  int slot = 0;
  for (int s = 0; s < NMAIN; ++s) {
    WAIT_V(2 * (G_R - 2));
    RAW_BARRIER();
    issue((slot + G_R - 1) & (G_R - 1));
    compute(slot);
    slot = (slot + 1) & (G_R - 1);
  }
#pragma unroll
  for (int t = 0; t < G_R - 1; ++t) {
    switch (G_R - 2 - t) {
      case 0: WAIT_V(0); break;
      case 1: WAIT_V(2); break;
      case 2: WAIT_V(4); break;
      case 3: WAIT_V(6); break;
      case 4: WAIT_V(8); break;
      case 5: WAIT_V(10); break;
      default: WAIT_V(12); break;
    }
    RAW_BARRIER();
    compute(slot);
    slot = (slot + 1) & (G_R - 1);
  }

  const int row = rt * 64 + rg * 16 + l15;
#pragma unroll
  for (int j = 0; j < 2; ++j) {
    const int col = ct * 64 + (2 * ch + j) * 16 + 4 * g;
    const u32x2 rv = *(const u32x2*)(res + (size_t)row * D_ + col);
    f32x4 v = acc[j];
    v[0] += bflo(rv[0]);
    v[1] += bfhi(rv[0]);
    v[2] += bflo(rv[1]);
    v[3] += bfhi(rv[1]);
    if (BIAS) {
      const u32x2 bv = *(const u32x2*)(bias + col);
      v[0] += bflo(bv[0]);
      v[1] += bfhi(bv[0]);
      v[2] += bflo(bv[1]);
      v[3] += bfhi(bv[1]);
    }
    u32x2 pk = {pack2(v[0], v[1]), pack2(v[2], v[3])};
    *(u32x2*)(out + (size_t)row * D_ + col) = pk;
  }
}

template <int K, bool BIAS>
__global__ __launch_bounds__(512, 2) void k_gemm64(const u16* __restrict__ A, const u16* __restrict__ W,
                                                   const u16* __restrict__ res, const u16* __restrict__ bias,
                                                   u16* __restrict__ out) {
  extern __shared__ __attribute__((aligned(1024))) char smem[];
  gemm64_body<K, BIAS>(A, W, res, bias, out, smem);
}

static constexpr int K3_STAGE = 32 * 1024;
static constexpr int K3_R = 4;
static constexpr int K3_STAT = K3_R * K3_STAGE;
static constexpr int K3_COLP = K3_STAT + 128 * 8;
static constexpr int K3_LDS = K3_COLP + 4 * 112 * 4;

__device__ __forceinline__ float gelu_tanh(float v) {
  const float u = 0.7978845608028654f * (v + 0.044715f * v * v * v);
  return v * __builtin_amdgcn_rcpf(1.f + __expf(-2.f * u));
}

template <int NT>
__device__ __forceinline__ void k3_body(const u16* __restrict__ b1, u16* __restrict__ h1, char* smem, int rg, int ch,
                                        int lane, int w, int pan, int ct, const char* (&src)[4]) {
  const int g = lane >> 4, l15 = lane & 15, q4 = l15 >> 2, p4 = lane & 3;
  auto issue = [&](int slot) {
    char* base = smem + slot * K3_STAGE + w * 1024;
    glds16(src[0], base);
    glds16(src[1], base + 8192);
    glds16(src[2], base + 16384);
    glds16(src[3], base + 24576);
    src[0] += 128;
    src[1] += 128;
    src[2] += 64 * F_ * 2;
    src[3] += 64 * F_ * 2;
  };
  const int mtA = 2 * rg + ch, mtB = 2 * rg + 1 - ch;
  const int aoff0 = l15 * 128 + ((g ^ (l15 >> 1)) * 16);
  const int boff = 16384 + g * 2048 + ch * 512 + q4 * 64 + p4 * 8;
  const int sw = (g & 1) * 32;

  f32x4 acc[2][NT], accC[NT], accR = {0, 0, 0, 0};
#pragma unroll
  for (int j = 0; j < NT; ++j) {
    acc[0][j] = f32x4{0, 0, 0, 0};
    acc[1][j] = f32x4{0, 0, 0, 0};
    accC[j] = f32x4{0, 0, 0, 0};
  }
  float ssq = 0.f;
  const bf16x8 ones = ones8();

#pragma unroll
  for (int s = 0; s < K3_R - 1; ++s) issue(s);

#pragma unroll
  for (int s = 0; s < 8; ++s) {
    const int rem = (7 - s) < (K3_R - 2) ? (7 - s) : (K3_R - 2);
    switch (rem) {
      case 0: WAIT_V(0); break;
      case 1: WAIT_V(4); break;
      default: WAIT_V(8); break;
    }
    RAW_BARRIER();
    if (s + K3_R - 1 < 8) issue((s + K3_R - 1) % K3_R);
    const char* st = smem + (s % K3_R) * K3_STAGE;
#pragma unroll
    for (int ks = 0; ks < 2; ++ks) {
      const int ao = aoff0 ^ (ks * 64);
      bf16x8 a0 = lds_a(st + mtA * 2048 + ao);
      bf16x8 a1 = lds_a(st + mtB * 2048 + ao);
      bf16x8 b[NT];
#pragma unroll
      for (int j = 0; j < NT; ++j) {
        const int o2 = boff + (j >> 1) * 256 + (((j & 1) * 32) ^ sw);
        b[j] = lds_b(st + o2 + (ks * 8 + 0) * 1024, st + o2 + (ks * 8 + 1) * 1024);
      }
#pragma unroll
      for (int j = 0; j < NT; ++j) {
        acc[0][j] = mfma16(b[j], a0, acc[0][j]);
        acc[1][j] = mfma16(b[j], a1, acc[1][j]);
      }
      accR = mfma16(ones, a0, accR);
      ssq = sumsq8(a0, ssq);
      if (((s * 2 + ks) & 3) == rg) {
#pragma unroll
        for (int j = 0; j < NT; ++j) accC[j] = mfma16(b[j], ones, accC[j]);
      }
    }
  }
  {
    float2* stat = (float2*)(smem + K3_STAT);
    float* colp = (float*)(smem + K3_COLP);
    ssq += __shfl_xor(ssq, 16);
    ssq += __shfl_xor(ssq, 32);
    if (lane < 16) {
      const float mean = accR[0] * (1.f / D_);
      float var = ssq * (1.f / D_) - mean * mean;
      var = var < 0.f ? 0.f : var;
      const float r1 = rsqrtf(var + EPS);
      const float v1 = var * r1 * r1;
      const float r2 = rsqrtf(v1 + EPS);
      stat[mtA * 16 + lane] = make_float2(mean, r1 * r2);
    }
    if (l15 == 0) {
#pragma unroll
      for (int j = 0; j < NT; ++j) *(f32x4*)(colp + rg * 112 + (4 * ch + j) * 16 + 4 * g) = accC[j];
    }
  }
  RAW_BARRIER();
  {
    const float2* stat = (const float2*)(smem + K3_STAT);
    const float* colp = (const float*)(smem + K3_COLP);
    const float2 sv0 = stat[mtA * 16 + l15], sv1 = stat[mtB * 16 + l15];
#pragma unroll
    for (int j = 0; j < NT; ++j) {
      const int c = (4 * ch + j) * 16 + 4 * g;
      const f32x4 cs = *(const f32x4*)(colp + c) + *(const f32x4*)(colp + 112 + c) + *(const f32x4*)(colp + 224 + c) +
                       *(const f32x4*)(colp + 336 + c);
      const u32x2 bv = *(const u32x2*)(b1 + ct * 112 + c);
      const f32x4 bb = {bflo(bv[0]), bfhi(bv[0]), bflo(bv[1]), bfhi(bv[1])};
#pragma unroll
      for (int i = 0; i < 2; ++i) {
        const float2 sv = i == 0 ? sv0 : sv1;
        const int row = pan * 128 + (i == 0 ? mtA : mtB) * 16 + l15;
        f32x4 v = (acc[i][j] - sv.x * cs) * sv.y + bb;
        u32x2 pk = {pack2(gelu_tanh(v[0]), gelu_tanh(v[1])), pack2(gelu_tanh(v[2]), gelu_tanh(v[3]))};
        *(u32x2*)(h1 + (size_t)row * F_ + ct * 112 + c) = pk;
      }
    }
  }
}

__global__ __launch_bounds__(512, 2) void k3_mlp1(const u16* __restrict__ h, const u16* __restrict__ w1,
                                                  const u16* __restrict__ b1, u16* __restrict__ h1) {
  extern __shared__ __attribute__((aligned(1024))) char smem[];
  const int tid = threadIdx.x, lane = tid & 63;
  const int w = __builtin_amdgcn_readfirstlane(tid >> 6);
  const int rg = w & 3, ch = w >> 2;
  const int bid = blockIdx.x;
  const int pan = (bid & 7) * 2 + (bid >> 7), ct = (bid >> 3) & 15;
  const int g = lane >> 4, l15 = lane & 15, q4 = l15 >> 2, p4 = lane & 3;
  const char* src[4];
#pragma unroll
  for (int i = 0; i < 2; ++i) {
    const int rl = 8 * (w + 8 * i) + (lane >> 3);
    const int chunk = (lane & 7) ^ ((rl >> 1) & 7);
    src[i] = (const char*)(h + (size_t)(pan * 128 + rl) * D_ + chunk * 8);
  }
#pragma unroll
  for (int i = 0; i < 2; ++i) {
    const int b = 4 * (w + 8 * i) + g;
    const int c4 = b >> 2, pr = b & 3;
    const int chunk = p4 ^ (2 * ((c4 >> 1) & 1));
    int colc = pr * 32 + chunk * 8;
    if (colc >= 112) colc -= 16;
    const int krow = 4 * c4 + q4;
    src[2 + i] = (const char*)(w1 + (size_t)krow * F_ + ct * 112 + colc);
  }
  if (ch == 0)
    k3_body<4>(b1, h1, smem, rg, ch, lane, w, pan, ct, src);
  else
    k3_body<3>(b1, h1, smem, rg, ch, lane, w, pan, ct, src);
}

extern "C" void kernel_launch(void* const* d_in, const int* in_sizes, int n_in, void* d_out, int out_size, void* d_ws,
                              size_t ws_size, hipStream_t stream) {
  const u16* x = (const u16*)d_in[0];
  const u16* wqkv = (const u16*)d_in[1];
  const u16* wo = (const u16*)d_in[2];
  const u16* w1 = (const u16*)d_in[4];
  const u16* b1 = (const u16*)d_in[5];
  const u16* w2 = (const u16*)d_in[6];
  const u16* b2 = (const u16*)d_in[7];
  u16* out = (u16*)d_out;
  char* ws = (char*)d_ws;
  u16* o = (u16*)ws;
  u16* h = (u16*)(ws + (size_t)2 * 1024 * 1024);
  u16* h1 = (u16*)(ws + (size_t)4 * 1024 * 1024);

  static_assert(K1_LDS <= 160 * 1024 && G_LDS <= 160 * 1024 && K3_LDS <= 160 * 1024, "lds");
  (void)hipFuncSetAttribute((const void*)k1_qkv_attn, hipFuncAttributeMaxDynamicSharedMemorySize, K1_LDS);
  (void)hipFuncSetAttribute((const void*)k_gemm64<512, false>, hipFuncAttributeMaxDynamicSharedMemorySize, G_LDS);
  (void)hipFuncSetAttribute((const void*)k3_mlp1, hipFuncAttributeMaxDynamicSharedMemorySize, K3_LDS);
  (void)hipFuncSetAttribute((const void*)k_gemm64<1792, true>, hipFuncAttributeMaxDynamicSharedMemorySize, G_LDS);

  k1_qkv_attn<<<256, 512, K1_LDS, stream>>>(x, wqkv, o);
  k_gemm64<512, false><<<256, 512, G_LDS, stream>>>(o, wo, x, b2, h);
  k3_mlp1<<<256, 512, K3_LDS, stream>>>(h, w1, b1, h1);
  k_gemm64<1792, true><<<256, 512, G_LDS, stream>>>(h1, w2, h, b2, out);
}
```

```cpp
#include <hip/hip_runtime.h>
#include <stdint.h>

typedef __attribute__((ext_vector_type(4))) short s16x4;
typedef __attribute__((ext_vector_type(8))) short s16x8;
typedef __attribute__((ext_vector_type(2))) __bf16 bf16x2;
typedef __attribute__((ext_vector_type(8))) __bf16 bf16x8;
typedef __attribute__((ext_vector_type(4))) float f32x4;
typedef __attribute__((ext_vector_type(4))) unsigned u32x4;
typedef __attribute__((ext_vector_type(2))) unsigned u32x2;
typedef unsigned short u16;
#define LDS_AS __attribute__((address_space(3)))

#define WAIT_V(n) asm volatile("s_waitcnt vmcnt(%0)" ::"n"(n) : "memory")
#define WAIT_L0() asm volatile("s_waitcnt lgkmcnt(0)" ::: "memory")
#define RAW_BARRIER()                  \
  do {                                 \
    WAIT_L0();                         \
    __builtin_amdgcn_s_barrier();      \
    asm volatile("" ::: "memory");     \
  } while (0)

#define STAMP(kid, i)
#define STAMPR(kid, i)
static constexpr int S_ = 2048;
static constexpr int D_ = 512;
static constexpr int F_ = 1792;
static constexpr float EPS = 1e-5f;

__device__ __forceinline__ unsigned pack2(float a, float b) {
  bf16x2 v = {(__bf16)a, (__bf16)b};
  return __builtin_bit_cast(unsigned, v);
}
__device__ __forceinline__ float bflo(unsigned u) { return __uint_as_float(u << 16); }
__device__ __forceinline__ float bfhi(unsigned u) { return __uint_as_float(u & 0xffff0000u); }

__device__ __forceinline__ f32x4 mfma16(bf16x8 a, bf16x8 b, f32x4 c) {
  return __builtin_amdgcn_mfma_f32_16x16x32_bf16(a, b, c, 0, 0, 0);
}
__device__ __forceinline__ bf16x8 lds_a(const char* p) { return *(const bf16x8*)p; }
__device__ __forceinline__ bf16x8 lds_b(const char* p0, const char* p1) {
  s16x4 a = __builtin_amdgcn_ds_read_tr16_b64_v4i16((LDS_AS s16x4*)p0);
  s16x4 b = __builtin_amdgcn_ds_read_tr16_b64_v4i16((LDS_AS s16x4*)p1);
  s16x8 f = __builtin_shufflevector(a, b, 0, 1, 2, 3, 4, 5, 6, 7);
  return __builtin_bit_cast(bf16x8, f);
}
__device__ __forceinline__ float sumsq8(bf16x8 a, float c) {
  bf16x2 p0 = __builtin_shufflevector(a, a, 0, 1);
  bf16x2 p1 = __builtin_shufflevector(a, a, 2, 3);
  bf16x2 p2 = __builtin_shufflevector(a, a, 4, 5);
  bf16x2 p3 = __builtin_shufflevector(a, a, 6, 7);
  c = __builtin_amdgcn_fdot2_f32_bf16(p0, p0, c, false);
  c = __builtin_amdgcn_fdot2_f32_bf16(p1, p1, c, false);
  c = __builtin_amdgcn_fdot2_f32_bf16(p2, p2, c, false);
  c = __builtin_amdgcn_fdot2_f32_bf16(p3, p3, c, false);
  return c;
}
__device__ __forceinline__ float dot2u(unsigned a, unsigned b, float c) {
  return __builtin_amdgcn_fdot2_f32_bf16(__builtin_bit_cast(bf16x2, a), __builtin_bit_cast(bf16x2, b), c, false);
}
__device__ __forceinline__ float dot8(u32x4 a, u32x4 b) {
  const unsigned a0 = a[0], a1 = a[1], a2 = a[2], a3 = a[3];
  const unsigned b0 = b[0], b1 = b[1], b2 = b[2], b3 = b[3];
  float c = 0.f;
  c = dot2u(a0, b0, c);
  c = dot2u(a1, b1, c);
  c = dot2u(a2, b2, c);
  c = dot2u(a3, b3, c);
  return c;
}
__device__ __forceinline__ bf16x8 ones8() {
  s16x8 o = {0x3F80, 0x3F80, 0x3F80, 0x3F80, 0x3F80, 0x3F80, 0x3F80, 0x3F80};
  return __builtin_bit_cast(bf16x8, o);
}
__device__ __forceinline__ void glds16(const void* g, char* l) {
  __builtin_amdgcn_global_load_lds((const unsigned*)g, (unsigned*)l, 16, 0, 0);
}

static constexpr int K1_STAGE = 30 * 1024;
static constexpr int K1_R = 4;
static constexpr int K1_ABYTES = 18 * 1024;
static constexpr int K1_STAT = K1_R * K1_STAGE;
static constexpr int K1_COLP = K1_STAT + 144 * 8;
static constexpr int K1_LDS = K1_COLP + 4 * 96 * 4;
static constexpr int QKV_PITCH = 208;

__device__ __forceinline__ void k1_body(const u16* __restrict__ x, const u16* __restrict__ wqkv, u16* __restrict__ o,
                                        char* smem) {
  const int tid = threadIdx.x, lane = tid & 63;
  const int w = __builtin_amdgcn_readfirstlane(tid >> 6);
  const int rg = w >> 1, ch = w & 1;
  const int bid = blockIdx.x;
  const int panel = (bid & 7) * 2 + (bid >> 7), hp = (bid >> 3) & 15;
  const int row0 = panel * 128 - 16;
  STAMPR(0, 14);
  STAMP(0, 0);
  const int g = lane >> 4, l15 = lane & 15, q4 = l15 >> 2, p4 = lane & 3;

  const char* src[4];
  int stride[4];
#pragma unroll
  for (int i = 0; i < 4; ++i) {
    const int p = w + 8 * i;
    if (p < 18) {
      const int rl = 8 * p + (lane >> 3);
      const int chunk = (lane & 7) ^ ((rl >> 1) & 7);
      int gr = row0 + rl;
      gr = gr < 0 ? 0 : gr;
      src[i] = (const char*)(x + (size_t)gr * D_ + chunk * 8);
      stride[i] = 128;
    } else {
      const int pb = (p < 30 ? p : 29) - 18;
      const int b = 4 * pb + g;
      const int c4 = b / 3, pr = b - 3 * c4;
      const int chunk = p4 ^ (2 * ((c4 >> 1) & 1));
      const int krow = 4 * c4 + q4;
      src[i] = (const char*)(wqkv + (size_t)krow * (3 * D_) + pr * D_ + hp * 32 + chunk * 8);
      stride[i] = 64 * 3 * D_ * 2;
    }
  }
  const bool has4 = (w < 6);
  auto issue = [&](int slot) {
    char* base = smem + slot * K1_STAGE + w * 1024;
#pragma unroll
    for (int i = 0; i < 3; ++i) {
      glds16(src[i], base + i * 8192);
      src[i] += stride[i];
    }
    if (has4) {
      glds16(src[3], base + 3 * 8192);
      src[3] += stride[3];
    }
  };

  const int mtA = 1 + 2 * rg + ch, mtB = 2 + 2 * rg - ch;
  const int aoff0 = l15 * 128 + ((g ^ (l15 >> 1)) * 16);
  const int boff = K1_ABYTES + g * 1536 + q4 * 64 + ((ch ^ (g & 1)) * 32) + p4 * 8;

  f32x4 acc[2][3], accH = {0, 0, 0, 0}, accR = {0, 0, 0, 0}, accRH = {0, 0, 0, 0}, accC[3];
#pragma unroll
  for (int j = 0; j < 3; ++j) {
    acc[0][j] = f32x4{0, 0, 0, 0};
    acc[1][j] = f32x4{0, 0, 0, 0};
    accC[j] = f32x4{0, 0, 0, 0};
  }
  float ssq = 0.f, ssqH = 0.f;
  const bf16x8 ones = ones8();

#pragma unroll
  for (int s = 0; s < K1_R - 1; ++s) issue(s);

#pragma unroll
  for (int s = 0; s < 8; ++s) {
    constexpr int dummy = 0;
    (void)dummy;
    const int rem = (7 - s) < (K1_R - 2) ? (7 - s) : (K1_R - 2);
    if (has4) {
      switch (rem) {
        case 0: WAIT_V(0); break;
        case 1: WAIT_V(4); break;
        default: WAIT_V(8); break;
      }
    } else {
      switch (rem) {
        case 0: WAIT_V(0); break;
        case 1: WAIT_V(3); break;
        default: WAIT_V(6); break;
      }
    }
    RAW_BARRIER();
    STAMP(0, 1 + s);
    if (s + K1_R - 1 < 8) issue((s + K1_R - 1) % K1_R);
    const char* st = smem + (s % K1_R) * K1_STAGE;
#pragma unroll
    for (int ks = 0; ks < 2; ++ks) {
      const int ao = aoff0 ^ (ks * 64);
      bf16x8 a0 = lds_a(st + mtA * 2048 + ao);
      bf16x8 a1 = lds_a(st + mtB * 2048 + ao);
      bf16x8 b[3];
#pragma unroll
      for (int j = 0; j < 3; ++j)
        b[j] = lds_b(st + boff + (ks * 8 + 0) * 768 + j * 256, st + boff + (ks * 8 + 1) * 768 + j * 256);
#pragma unroll
      for (int j = 0; j < 3; ++j) {
        acc[0][j] = mfma16(b[j], a0, acc[0][j]);
        acc[1][j] = mfma16(b[j], a1, acc[1][j]);
      }
      accR = mfma16(ones, a0, accR);
      ssq = sumsq8(a0, ssq);
      if (rg < 2) {
        bf16x8 ah = lds_a(st + ao);
        accH = mfma16(rg == 0 ? b[1] : b[2], ah, accH);
        if (w == 0) {
          accRH = mfma16(ones, ah, accRH);
          ssqH = sumsq8(ah, ssqH);
        }
      }
      if (((s * 2 + ks) & 3) == rg) {
#pragma unroll
        for (int j = 0; j < 3; ++j) accC[j] = mfma16(b[j], ones, accC[j]);
      }
    }
  }

  STAMP(0, 9);
  {
    float2* stat = (float2*)(smem + K1_STAT);
    float* colp = (float*)(smem + K1_COLP);
    ssq += __shfl_xor(ssq, 16);
    ssq += __shfl_xor(ssq, 32);
    if (lane < 16) {
      const float mean = accR[0] * (1.f / D_);
      float var = ssq * (1.f / D_) - mean * mean;
      var = var < 0.f ? 0.f : var;
      stat[mtA * 16 + lane] = make_float2(mean, rsqrtf(var + EPS));
    }
    if (w == 0) {
      ssqH += __shfl_xor(ssqH, 16);
      ssqH += __shfl_xor(ssqH, 32);
      if (lane < 16) {
        const float mean = accRH[0] * (1.f / D_);
        float var = ssqH * (1.f / D_) - mean * mean;
        var = var < 0.f ? 0.f : var;
        stat[lane] = make_float2(mean, rsqrtf(var + EPS));
      }
    }
    if (l15 == 0) {
#pragma unroll
      for (int j = 0; j < 3; ++j) *(f32x4*)(colp + rg * 96 + (2 * j + ch) * 16 + 4 * g) = accC[j];
    }
  }
  RAW_BARRIER();
  {
    const float2* stat = (const float2*)(smem + K1_STAT);
    const float* colp = (const float*)(smem + K1_COLP);
    f32x4 cs[3];
#pragma unroll
    for (int j = 0; j < 3; ++j) {
      const int c = (2 * j + ch) * 16 + 4 * g;
      cs[j] = *(const f32x4*)(colp + c) + *(const f32x4*)(colp + 96 + c) + *(const f32x4*)(colp + 192 + c) +
              *(const f32x4*)(colp + 288 + c);
    }
#pragma unroll
    for (int i = 0; i < 2; ++i) {
      const int rl = (i == 0 ? mtA : mtB) * 16 + l15;
      const float2 sv = stat[rl];
#pragma unroll
      for (int j = 0; j < 3; ++j) {
        f32x4 v = (acc[i][j] - sv.x * cs[j]) * sv.y;
        u32x2 pk = {pack2(v[0], v[1]), pack2(v[2], v[3])};
        *(u32x2*)(smem + rl * QKV_PITCH + ((2 * j + ch) * 16 + 4 * g) * 2) = pk;
      }
    }
    if (rg < 2) {
      const float2 sv = stat[l15];
      const int j = 1 + rg;
      f32x4 c = (rg == 0) ? cs[1] : cs[2];
      f32x4 v = (accH - sv.x * c) * sv.y;
      u32x2 pk = {pack2(v[0], v[1]), pack2(v[2], v[3])};
      *(u32x2*)(smem + l15 * QKV_PITCH + ((2 * j + ch) * 16 + 4 * g) * 2) = pk;
    }
  }
  RAW_BARRIER();
  STAMP(0, 10);
  {
    const int half = tid & 1, head = (tid >> 1) & 1, r = tid >> 2;
    const char* base = smem + (r + 16) * QKV_PITCH + (head * 16 + half * 8) * 2;
    const u32x4 qv = *(const u32x4*)base;
    const int grow = panel * 128 + r;
    float lg[8];
#pragma unroll
    for (int wi = 0; wi < 8; ++wi) {
      const u32x4 kv = *(const u32x4*)(base - 2 * wi * QKV_PITCH + 64);
      float d = dot8(qv, kv);
      d += __shfl_xor(d, 1);
      lg[wi] = (grow - 2 * wi >= 0) ? d * 0.25f : -1e9f;
    }
    float mx = lg[0];
#pragma unroll
    for (int wi = 1; wi < 8; ++wi) mx = fmaxf(mx, lg[wi]);
    float sum = 0.f;
#pragma unroll
    for (int wi = 0; wi < 8; ++wi) {
      lg[wi] = __expf(lg[wi] - mx);
      sum += lg[wi];
    }
    const float inv = __builtin_amdgcn_rcpf(sum);
    float ov[8];
#pragma unroll
    for (int d = 0; d < 8; ++d) ov[d] = 0.f;
#pragma unroll
    for (int wi = 0; wi < 8; ++wi) {
      const u32x4 vv = *(const u32x4*)(base - 2 * wi * QKV_PITCH + 128);
      const float p = lg[wi];
#pragma unroll
      for (int d = 0; d < 4; ++d) {
        ov[2 * d] = fmaf(p, bflo(vv[d]), ov[2 * d]);
        ov[2 * d + 1] = fmaf(p, bfhi(vv[d]), ov[2 * d + 1]);
      }
    }
    u32x4 out;
#pragma unroll
    for (int d = 0; d < 4; ++d) out[d] = pack2(ov[2 * d] * inv, ov[2 * d + 1] * inv);
    *(u32x4*)(o + (size_t)grow * D_ + hp * 32 + head * 16 + half * 8) = out;
  }
  STAMP(0, 11);
  STAMPR(0, 15);
}

__global__ __launch_bounds__(512, 2) void k1_qkv_attn(const u16* __restrict__ x, const u16* __restrict__ wqkv,
                                                      u16* __restrict__ o) {
  extern __shared__ __attribute__((aligned(1024))) char smem[];
  k1_body(x, wqkv, o, smem);
}

static constexpr int G_STAGE = 16 * 1024;
static constexpr int G_R = 8;
static constexpr int G_LDS = G_R * G_STAGE;

template <int K, bool BIAS>
__device__ __forceinline__ void gemm64_body(const u16* __restrict__ A, const u16* __restrict__ W,
                                            const u16* __restrict__ res, const u16* __restrict__ bias,
                                            u16* __restrict__ out, char* smem) {
  constexpr int NS = K / 64;
  constexpr int KID = BIAS ? 3 : 1;
  STAMPR(KID, 14);
  STAMP(KID, 0);
  const int tid = threadIdx.x, lane = tid & 63;
  const int w = __builtin_amdgcn_readfirstlane(tid >> 6);
  const int rg = w & 3, ch = w >> 2;
  const int bid = blockIdx.x;
  const int rt = (bid & 7) * 4 + (bid >> 6), ct = (bid >> 3) & 7;
  const int g = lane >> 4, l15 = lane & 15, q4 = l15 >> 2, p4 = lane & 3;

  const char *srcA, *srcB;
  {
    const int rl = 8 * w + (lane >> 3);
    const int chunk = (lane & 7) ^ ((rl >> 1) & 7);
    srcA = (const char*)(A + (size_t)(rt * 64 + rl) * K + chunk * 8);
    const int b = 4 * w + g;
    const int c4 = b >> 1, pr = b & 1;
    const int chunkb = p4 ^ (2 * ((c4 >> 1) & 1));
    const int krow = 4 * c4 + q4;
    srcB = (const char*)(W + (size_t)krow * D_ + ct * 64 + pr * 32 + chunkb * 8);
  }
  auto issue = [&](int slot) {
    char* base = smem + slot * G_STAGE + w * 1024;
    glds16(srcA, base);
    glds16(srcB, base + 8192);
    srcA += 128;
    srcB += 64 * D_ * 2;
  };
  const int aoff0 = rg * 2048 + l15 * 128 + ((g ^ (l15 >> 1)) * 16);
  const int boff = 8192 + g * 1024 + ch * 256 + q4 * 64 + p4 * 8;
  const int sw = (g & 1) * 32;

  f32x4 acc[2] = {f32x4{0, 0, 0, 0}, f32x4{0, 0, 0, 0}};

#pragma unroll
  for (int s = 0; s < G_R - 1; ++s) issue(s);

  auto compute = [&](int slot) {
    const char* st = smem + slot * G_STAGE;
#pragma unroll
    for (int ks = 0; ks < 2; ++ks) {
      bf16x8 a = lds_a(st + (aoff0 ^ (ks * 64)));
#pragma unroll
      for (int j = 0; j < 2; ++j) {
        const int o2 = boff + ((j * 32) ^ sw);
        bf16x8 b = lds_b(st + o2 + (ks * 8 + 0) * 512, st + o2 + (ks * 8 + 1) * 512);
        acc[j] = mfma16(b, a, acc[j]);
      }
    }
  };

  STAMP(KID, 1);
  constexpr int NMAIN = NS - (G_R - 1);
  int slot = 0;
  for (int s = 0; s < NMAIN; ++s) {
    WAIT_V(2 * (G_R - 2));
    RAW_BARRIER();
    if (s == 0) STAMP(KID, 2);
    issue((slot + G_R - 1) & (G_R - 1));
    compute(slot);
    slot = (slot + 1) & (G_R - 1);
  }
#pragma unroll
  for (int t = 0; t < G_R - 1; ++t) {
    switch (G_R - 2 - t) {
      case 0: WAIT_V(0); break;
      case 1: WAIT_V(2); break;
      case 2: WAIT_V(4); break;
      case 3: WAIT_V(6); break;
      case 4: WAIT_V(8); break;
      case 5: WAIT_V(10); break;
      default: WAIT_V(12); break;
    }
    RAW_BARRIER();
    compute(slot);
    slot = (slot + 1) & (G_R - 1);
  }

  STAMP(KID, 3);
  const int row = rt * 64 + rg * 16 + l15;
#pragma unroll
  for (int j = 0; j < 2; ++j) {
    const int col = ct * 64 + (2 * ch + j) * 16 + 4 * g;
    const u32x2 rv = *(const u32x2*)(res + (size_t)row * D_ + col);
    f32x4 v = acc[j];
    v[0] += bflo(rv[0]);
    v[1] += bfhi(rv[0]);
    v[2] += bflo(rv[1]);
    v[3] += bfhi(rv[1]);
    if (BIAS) {
      const u32x2 bv = *(const u32x2*)(bias + col);
      v[0] += bflo(bv[0]);
      v[1] += bfhi(bv[0]);
      v[2] += bflo(bv[1]);
      v[3] += bfhi(bv[1]);
    }
    u32x2 pk = {pack2(v[0], v[1]), pack2(v[2], v[3])};
    *(u32x2*)(out + (size_t)row * D_ + col) = pk;
  }
  STAMP(KID, 4);
  STAMPR(KID, 15);
}

template <int K, bool BIAS>
__global__ __launch_bounds__(512, 2) void k_gemm64(const u16* __restrict__ A, const u16* __restrict__ W,
                                                   const u16* __restrict__ res, const u16* __restrict__ bias,
                                                   u16* __restrict__ out) {
  extern __shared__ __attribute__((aligned(1024))) char smem[];
  gemm64_body<K, BIAS>(A, W, res, bias, out, smem);
}

static constexpr int K3_STAGE = 32 * 1024;
static constexpr int K3_R = 4;
static constexpr int K3_STAT = K3_R * K3_STAGE;
static constexpr int K3_COLP = K3_STAT + 128 * 8;
static constexpr int K3_LDS = K3_COLP + 4 * 112 * 4;

__device__ __forceinline__ float gelu_tanh(float v) {
  const float u = 0.7978845608028654f * (v + 0.044715f * v * v * v);
  return v * __builtin_amdgcn_rcpf(1.f + __expf(-2.f * u));
}

template <int NT>
__device__ __forceinline__ void k3_body(const u16* __restrict__ b1, u16* __restrict__ h1, char* smem, int rg, int ch,
                                        int lane, int w, int pan, int ct, const char* (&src)[4]) {
  const int g = lane >> 4, l15 = lane & 15, q4 = l15 >> 2, p4 = lane & 3;
  auto issue = [&](int slot) {
    char* base = smem + slot * K3_STAGE + w * 1024;
    glds16(src[0], base);
    glds16(src[1], base + 8192);
    glds16(src[2], base + 16384);
    glds16(src[3], base + 24576);
    src[0] += 128;
    src[1] += 128;
    src[2] += 64 * F_ * 2;
    src[3] += 64 * F_ * 2;
  };
  STAMPR(2, 14);
  STAMP(2, 0);
  const int mtA = 2 * rg + ch, mtB = 2 * rg + 1 - ch;
  const int aoff0 = l15 * 128 + ((g ^ (l15 >> 1)) * 16);
  const int boff = 16384 + g * 2048 + ch * 512 + q4 * 64 + p4 * 8;
  const int sw = (g & 1) * 32;

  f32x4 acc[2][NT], accC[NT], accR = {0, 0, 0, 0};
#pragma unroll
  for (int j = 0; j < NT; ++j) {
    acc[0][j] = f32x4{0, 0, 0, 0};
    acc[1][j] = f32x4{0, 0, 0, 0};
    accC[j] = f32x4{0, 0, 0, 0};
  }
  float ssq = 0.f;
  const bf16x8 ones = ones8();

#pragma unroll
  for (int s = 0; s < K3_R - 1; ++s) issue(s);

#pragma unroll
  for (int s = 0; s < 8; ++s) {
    const int rem = (7 - s) < (K3_R - 2) ? (7 - s) : (K3_R - 2);
    switch (rem) {
      case 0: WAIT_V(0); break;
      case 1: WAIT_V(4); break;
      default: WAIT_V(8); break;
    }
    RAW_BARRIER();
    STAMP(2, 1 + s);
    if (s + K3_R - 1 < 8) issue((s + K3_R - 1) % K3_R);
    const char* st = smem + (s % K3_R) * K3_STAGE;
#pragma unroll
    for (int ks = 0; ks < 2; ++ks) {
      const int ao = aoff0 ^ (ks * 64);
      bf16x8 a0 = lds_a(st + mtA * 2048 + ao);
      bf16x8 a1 = lds_a(st + mtB * 2048 + ao);
      bf16x8 b[NT];
#pragma unroll
      for (int j = 0; j < NT; ++j) {
        const int o2 = boff + (j >> 1) * 256 + (((j & 1) * 32) ^ sw);
        b[j] = lds_b(st + o2 + (ks * 8 + 0) * 1024, st + o2 + (ks * 8 + 1) * 1024);
      }
#pragma unroll
      for (int j = 0; j < NT; ++j) {
        acc[0][j] = mfma16(b[j], a0, acc[0][j]);
        acc[1][j] = mfma16(b[j], a1, acc[1][j]);
      }
      accR = mfma16(ones, a0, accR);
      ssq = sumsq8(a0, ssq);
      if (((s * 2 + ks) & 3) == rg) {
#pragma unroll
        for (int j = 0; j < NT; ++j) accC[j] = mfma16(b[j], ones, accC[j]);
      }
    }
  }
  STAMP(2, 9);
  {
    float2* stat = (float2*)(smem + K3_STAT);
    float* colp = (float*)(smem + K3_COLP);
    ssq += __shfl_xor(ssq, 16);
    ssq += __shfl_xor(ssq, 32);
    if (lane < 16) {
      const float mean = accR[0] * (1.f / D_);
      float var = ssq * (1.f / D_) - mean * mean;
      var = var < 0.f ? 0.f : var;
      const float r1 = rsqrtf(var + EPS);
      const float v1 = var * r1 * r1;
      const float r2 = rsqrtf(v1 + EPS);
      stat[mtA * 16 + lane] = make_float2(mean, r1 * r2);
    }
    if (l15 == 0) {
#pragma unroll
      for (int j = 0; j < NT; ++j) *(f32x4*)(colp + rg * 112 + (4 * ch + j) * 16 + 4 * g) = accC[j];
    }
  }
  RAW_BARRIER();
  {
    const float2* stat = (const float2*)(smem + K3_STAT);
    const float* colp = (const float*)(smem + K3_COLP);
    const float2 sv0 = stat[mtA * 16 + l15], sv1 = stat[mtB * 16 + l15];
#pragma unroll
    for (int j = 0; j < NT; ++j) {
      const int c = (4 * ch + j) * 16 + 4 * g;
      const f32x4 cs = *(const f32x4*)(colp + c) + *(const f32x4*)(colp + 112 + c) + *(const f32x4*)(colp + 224 + c) +
                       *(const f32x4*)(colp + 336 + c);
      const u32x2 bv = *(const u32x2*)(b1 + ct * 112 + c);
      const f32x4 bb = {bflo(bv[0]), bfhi(bv[0]), bflo(bv[1]), bfhi(bv[1])};
#pragma unroll
      for (int i = 0; i < 2; ++i) {
        const float2 sv = i == 0 ? sv0 : sv1;
        const int row = pan * 128 + (i == 0 ? mtA : mtB) * 16 + l15;
        f32x4 v = (acc[i][j] - sv.x * cs) * sv.y + bb;
        u32x2 pk = {pack2(gelu_tanh(v[0]), gelu_tanh(v[1])), pack2(gelu_tanh(v[2]), gelu_tanh(v[3]))};
        *(u32x2*)(h1 + (size_t)row * F_ + ct * 112 + c) = pk;
      }
    }
  }
  STAMP(2, 10);
  STAMPR(2, 15);
}

__global__ __launch_bounds__(512, 2) void k3_mlp1(const u16* __restrict__ h, const u16* __restrict__ w1,
                                                  const u16* __restrict__ b1, u16* __restrict__ h1) {
  extern __shared__ __attribute__((aligned(1024))) char smem[];
  const int tid = threadIdx.x, lane = tid & 63;
  const int w = __builtin_amdgcn_readfirstlane(tid >> 6);
  const int rg = w & 3, ch = w >> 2;
  const int bid = blockIdx.x;
  const int pan = (bid & 7) * 2 + (bid >> 7), ct = (bid >> 3) & 15;
  const int g = lane >> 4, l15 = lane & 15, q4 = l15 >> 2, p4 = lane & 3;
  const char* src[4];
#pragma unroll
  for (int i = 0; i < 2; ++i) {
    const int rl = 8 * (w + 8 * i) + (lane >> 3);
    const int chunk = (lane & 7) ^ ((rl >> 1) & 7);
    src[i] = (const char*)(h + (size_t)(pan * 128 + rl) * D_ + chunk * 8);
  }
#pragma unroll
  for (int i = 0; i < 2; ++i) {
    const int b = 4 * (w + 8 * i) + g;
    const int c4 = b >> 2, pr = b & 3;
    const int chunk = p4 ^ (2 * ((c4 >> 1) & 1));
    int colc = pr * 32 + chunk * 8;
    if (colc >= 112) colc -= 16;
    const int krow = 4 * c4 + q4;
    src[2 + i] = (const char*)(w1 + (size_t)krow * F_ + ct * 112 + colc);
  }
  if (ch == 0)
    k3_body<4>(b1, h1, smem, rg, ch, lane, w, pan, ct, src);
  else
    k3_body<3>(b1, h1, smem, rg, ch, lane, w, pan, ct, src);
}

extern "C" void kernel_launch(void* const* d_in, const int* in_sizes, int n_in, void* d_out, int out_size, void* d_ws,
                              size_t ws_size, hipStream_t stream) {
  const u16* x = (const u16*)d_in[0];
  const u16* wqkv = (const u16*)d_in[1];
  const u16* wo = (const u16*)d_in[2];
  const u16* w1 = (const u16*)d_in[4];
  const u16* b1 = (const u16*)d_in[5];
  const u16* w2 = (const u16*)d_in[6];
  const u16* b2 = (const u16*)d_in[7];
  u16* out = (u16*)d_out;
  char* ws = (char*)d_ws;
  u16* o = (u16*)ws;
  u16* h = (u16*)(ws + (size_t)2 * 1024 * 1024);
  u16* h1 = (u16*)(ws + (size_t)4 * 1024 * 1024);

  static_assert(K1_LDS <= 160 * 1024 && G_LDS <= 160 * 1024 && K3_LDS <= 160 * 1024, "lds");
  (void)hipFuncSetAttribute((const void*)k1_qkv_attn, hipFuncAttributeMaxDynamicSharedMemorySize, K1_LDS);
  (void)hipFuncSetAttribute((const void*)k_gemm64<512, false>, hipFuncAttributeMaxDynamicSharedMemorySize, G_LDS);
  (void)hipFuncSetAttribute((const void*)k3_mlp1, hipFuncAttributeMaxDynamicSharedMemorySize, K3_LDS);
  (void)hipFuncSetAttribute((const void*)k_gemm64<1792, true>, hipFuncAttributeMaxDynamicSharedMemorySize, G_LDS);

  k1_qkv_attn<<<256, 512, K1_LDS, stream>>>(x, wqkv, o);
  k_gemm64<512, false><<<256, 512, G_LDS, stream>>>(o, wo, x, b2, h);
  k3_mlp1<<<256, 512, K3_LDS, stream>>>(h, w1, b1, h1);
  k_gemm64<1792, true><<<256, 512, G_LDS, stream>>>(h1, w2, h, b2, out);
}
```

```cpp
#include <hip/hip_runtime.h>
#include <stdint.h>

typedef __attribute__((ext_vector_type(4))) short s16x4;
typedef __attribute__((ext_vector_type(8))) short s16x8;
typedef __attribute__((ext_vector_type(2))) __bf16 bf16x2;
typedef __attribute__((ext_vector_type(8))) __bf16 bf16x8;
typedef __attribute__((ext_vector_type(4))) float f32x4;
typedef __attribute__((ext_vector_type(4))) unsigned u32x4;
typedef __attribute__((ext_vector_type(2))) unsigned u32x2;
typedef unsigned short u16;
#define LDS_AS __attribute__((address_space(3)))

#define WAIT_V(n) asm volatile("s_waitcnt vmcnt(%0)" ::"n"(n) : "memory")
#define WAIT_L0() asm volatile("s_waitcnt lgkmcnt(0)" ::: "memory")
#define RAW_BARRIER()                  \
  do {                                 \
    WAIT_L0();                         \
    __builtin_amdgcn_s_barrier();      \
    asm volatile("" ::: "memory");     \
  } while (0)

#define STAMP(kid, i)
#define STAMPR(kid, i)
static constexpr int S_ = 2048;
static constexpr int D_ = 512;
static constexpr int F_ = 1792;
static constexpr float EPS = 1e-5f;

__device__ __forceinline__ unsigned pack2(float a, float b) {
  bf16x2 v = {(__bf16)a, (__bf16)b};
  return __builtin_bit_cast(unsigned, v);
}
__device__ __forceinline__ float bflo(unsigned u) { return __uint_as_float(u << 16); }
__device__ __forceinline__ float bfhi(unsigned u) { return __uint_as_float(u & 0xffff0000u); }

__device__ __forceinline__ f32x4 mfma16(bf16x8 a, bf16x8 b, f32x4 c) {
  return __builtin_amdgcn_mfma_f32_16x16x32_bf16(a, b, c, 0, 0, 0);
}
__device__ __forceinline__ bf16x8 lds_a(const char* p) { return *(const bf16x8*)p; }
__device__ __forceinline__ bf16x8 lds_b(const char* p0, const char* p1) {
  s16x4 a = __builtin_amdgcn_ds_read_tr16_b64_v4i16((LDS_AS s16x4*)p0);
  s16x4 b = __builtin_amdgcn_ds_read_tr16_b64_v4i16((LDS_AS s16x4*)p1);
  s16x8 f = __builtin_shufflevector(a, b, 0, 1, 2, 3, 4, 5, 6, 7);
  return __builtin_bit_cast(bf16x8, f);
}
__device__ __forceinline__ float sumsq8(bf16x8 a, float c) {
  bf16x2 p0 = __builtin_shufflevector(a, a, 0, 1);
  bf16x2 p1 = __builtin_shufflevector(a, a, 2, 3);
  bf16x2 p2 = __builtin_shufflevector(a, a, 4, 5);
  bf16x2 p3 = __builtin_shufflevector(a, a, 6, 7);
  c = __builtin_amdgcn_fdot2_f32_bf16(p0, p0, c, false);
  c = __builtin_amdgcn_fdot2_f32_bf16(p1, p1, c, false);
  c = __builtin_amdgcn_fdot2_f32_bf16(p2, p2, c, false);
  c = __builtin_amdgcn_fdot2_f32_bf16(p3, p3, c, false);
  return c;
}
__device__ __forceinline__ float dot2u(unsigned a, unsigned b, float c) {
  return __builtin_amdgcn_fdot2_f32_bf16(__builtin_bit_cast(bf16x2, a), __builtin_bit_cast(bf16x2, b), c, false);
}
__device__ __forceinline__ float dot8(u32x4 a, u32x4 b) {
  const unsigned a0 = a[0], a1 = a[1], a2 = a[2], a3 = a[3];
  const unsigned b0 = b[0], b1 = b[1], b2 = b[2], b3 = b[3];
  float c = 0.f;
  c = dot2u(a0, b0, c);
  c = dot2u(a1, b1, c);
  c = dot2u(a2, b2, c);
  c = dot2u(a3, b3, c);
  return c;
}
__device__ __forceinline__ bf16x8 ones8() {
  s16x8 o = {0x3F80, 0x3F80, 0x3F80, 0x3F80, 0x3F80, 0x3F80, 0x3F80, 0x3F80};
  return __builtin_bit_cast(bf16x8, o);
}
__device__ __forceinline__ void glds16(const void* g, char* l) {
  __builtin_amdgcn_global_load_lds((const unsigned*)g, (unsigned*)l, 16, 0, 0);
}

static constexpr int K1_STAGE = 30 * 1024;
static constexpr int K1_R = 4;
static constexpr int K1_ABYTES = 18 * 1024;
static constexpr int K1_STAT = K1_R * K1_STAGE;
static constexpr int K1_COLP = K1_STAT + 144 * 8;
static constexpr int K1_LDS = K1_COLP + 4 * 96 * 4;
static constexpr int QKV_PITCH = 208;

__device__ __forceinline__ void k1_body(const u16* __restrict__ x, const u16* __restrict__ wqkv, u16* __restrict__ o,
                                        const u16* __restrict__ wo, const u16* __restrict__ w1,
                                        const u16* __restrict__ w2, const u16* __restrict__ outp, char* smem) {
  const int tid = threadIdx.x, lane = tid & 63;
  const int w = __builtin_amdgcn_readfirstlane(tid >> 6);
  const int rg = w >> 1, ch = w & 1;
  const int bid = blockIdx.x;
  const int panel = (bid & 7) * 2 + (bid >> 7), hp = (bid >> 3) & 15;
  const int row0 = panel * 128 - 16;
  STAMPR(0, 14);
  STAMP(0, 0);
  const int g = lane >> 4, l15 = lane & 15, q4 = l15 >> 2, p4 = lane & 3;

  const char* src[4];
  int stride[4];
#pragma unroll
  for (int i = 0; i < 4; ++i) {
    const int p = w + 8 * i;
    if (p < 18) {
      const int rl = 8 * p + (lane >> 3);
      const int chunk = (lane & 7) ^ ((rl >> 1) & 7);
      int gr = row0 + rl;
      gr = gr < 0 ? 0 : gr;
      src[i] = (const char*)(x + (size_t)gr * D_ + chunk * 8);
      stride[i] = 128;
    } else {
      const int pb = (p < 30 ? p : 29) - 18;
      const int b = 4 * pb + g;
      const int c4 = b / 3, pr = b - 3 * c4;
      const int chunk = p4 ^ (2 * ((c4 >> 1) & 1));
      const int krow = 4 * c4 + q4;
      src[i] = (const char*)(wqkv + (size_t)krow * (3 * D_) + pr * D_ + hp * 32 + chunk * 8);
      stride[i] = 64 * 3 * D_ * 2;
    }
  }
  const bool has4 = (w < 6);
  auto issue = [&](int slot) {
    char* base = smem + slot * K1_STAGE + w * 1024;
#pragma unroll
    for (int i = 0; i < 3; ++i) {
      glds16(src[i], base + i * 8192);
      src[i] += stride[i];
    }
    if (has4) {
      glds16(src[3], base + 3 * 8192);
      src[3] += stride[3];
    }
  };

  const int mtA = 1 + 2 * rg + ch, mtB = 2 + 2 * rg - ch;
  const int aoff0 = l15 * 128 + ((g ^ (l15 >> 1)) * 16);
  const int boff = K1_ABYTES + g * 1536 + q4 * 64 + ((ch ^ (g & 1)) * 32) + p4 * 8;

  f32x4 acc[2][3], accH = {0, 0, 0, 0}, accR = {0, 0, 0, 0}, accRH = {0, 0, 0, 0}, accC[3];
#pragma unroll
  for (int j = 0; j < 3; ++j) {
    acc[0][j] = f32x4{0, 0, 0, 0};
    acc[1][j] = f32x4{0, 0, 0, 0};
    accC[j] = f32x4{0, 0, 0, 0};
  }
  float ssq = 0.f, ssqH = 0.f;
  const bf16x8 ones = ones8();

#pragma unroll
  for (int s = 0; s < K1_R - 1; ++s) issue(s);

#pragma unroll
  for (int s = 0; s < 8; ++s) {
    constexpr int dummy = 0;
    (void)dummy;
    const int rem = (7 - s) < (K1_R - 2) ? (7 - s) : (K1_R - 2);
    if (has4) {
      switch (rem) {
        case 0: WAIT_V(0); break;
        case 1: WAIT_V(4); break;
        default: WAIT_V(8); break;
      }
    } else {
      switch (rem) {
        case 0: WAIT_V(0); break;
        case 1: WAIT_V(3); break;
        default: WAIT_V(6); break;
      }
    }
    RAW_BARRIER();
    STAMP(0, 1 + s);
    if (s + K1_R - 1 < 8) issue((s + K1_R - 1) % K1_R);
    const char* st = smem + (s % K1_R) * K1_STAGE;
#pragma unroll
    for (int ks = 0; ks < 2; ++ks) {
      const int ao = aoff0 ^ (ks * 64);
      bf16x8 a0 = lds_a(st + mtA * 2048 + ao);
      bf16x8 a1 = lds_a(st + mtB * 2048 + ao);
      bf16x8 b[3];
#pragma unroll
      for (int j = 0; j < 3; ++j)
        b[j] = lds_b(st + boff + (ks * 8 + 0) * 768 + j * 256, st + boff + (ks * 8 + 1) * 768 + j * 256);
#pragma unroll
      for (int j = 0; j < 3; ++j) {
        acc[0][j] = mfma16(b[j], a0, acc[0][j]);
        acc[1][j] = mfma16(b[j], a1, acc[1][j]);
      }
      accR = mfma16(ones, a0, accR);
      ssq = sumsq8(a0, ssq);
      if (rg < 2) {
        bf16x8 ah = lds_a(st + ao);
        accH = mfma16(rg == 0 ? b[1] : b[2], ah, accH);
        if (w == 0) {
          accRH = mfma16(ones, ah, accRH);
          ssqH = sumsq8(ah, ssqH);
        }
      }
      if (((s * 2 + ks) & 3) == rg) {
#pragma unroll
        for (int j = 0; j < 3; ++j) accC[j] = mfma16(b[j], ones, accC[j]);
      }
    }
  }

  STAMP(0, 9);
  u32x4 wm0, wm1, wm2;
  {
    const int t16 = (bid * 512 + tid) * 16;
    const char* p0 = (t16 < 1792 * 1024) ? (const char*)w1 + t16 : (const char*)wo + (t16 - 1792 * 1024) ;
    const char* p1 = (t16 < 1792 * 1024) ? (const char*)w2 + t16 : (const char*)wo + (t16 - 1792 * 1024) + 256 * 1024;
    wm0 = *(const u32x4*)p0;
    wm1 = *(const u32x4*)p1;
    const int j = (bid >> 3) * 8 + (tid & 7);
    const char* p2 = (j < 176) ? (const char*)o + (size_t)(2 * 1024 * 1024) + (size_t)j * 65536 : (const char*)outp + (size_t)(j - 176) * 16384;
    wm2 = u32x4{0, 0, 0, 0};
    if (tid < 8) wm2 = *(const u32x4*)p2;
  }
  {
    float2* stat = (float2*)(smem + K1_STAT);
    float* colp = (float*)(smem + K1_COLP);
    ssq += __shfl_xor(ssq, 16);
    ssq += __shfl_xor(ssq, 32);
    if (lane < 16) {
      const float mean = accR[0] * (1.f / D_);
      float var = ssq * (1.f / D_) - mean * mean;
      var = var < 0.f ? 0.f : var;
      stat[mtA * 16 + lane] = make_float2(mean, rsqrtf(var + EPS));
    }
    if (w == 0) {
      ssqH += __shfl_xor(ssqH, 16);
      ssqH += __shfl_xor(ssqH, 32);
      if (lane < 16) {
        const float mean = accRH[0] * (1.f / D_);
        float var = ssqH * (1.f / D_) - mean * mean;
        var = var < 0.f ? 0.f : var;
        stat[lane] = make_float2(mean, rsqrtf(var + EPS));
      }
    }
    if (l15 == 0) {
#pragma unroll
      for (int j = 0; j < 3; ++j) *(f32x4*)(colp + rg * 96 + (2 * j + ch) * 16 + 4 * g) = accC[j];
    }
  }
  RAW_BARRIER();
  {
    const float2* stat = (const float2*)(smem + K1_STAT);
    const float* colp = (const float*)(smem + K1_COLP);
    f32x4 cs[3];
#pragma unroll
    for (int j = 0; j < 3; ++j) {
      const int c = (2 * j + ch) * 16 + 4 * g;
      cs[j] = *(const f32x4*)(colp + c) + *(const f32x4*)(colp + 96 + c) + *(const f32x4*)(colp + 192 + c) +
              *(const f32x4*)(colp + 288 + c);
    }
#pragma unroll
    for (int i = 0; i < 2; ++i) {
      const int rl = (i == 0 ? mtA : mtB) * 16 + l15;
      const float2 sv = stat[rl];
#pragma unroll
      for (int j = 0; j < 3; ++j) {
        f32x4 v = (acc[i][j] - sv.x * cs[j]) * sv.y;
        u32x2 pk = {pack2(v[0], v[1]), pack2(v[2], v[3])};
        *(u32x2*)(smem + rl * QKV_PITCH + ((2 * j + ch) * 16 + 4 * g) * 2) = pk;
      }
    }
    if (rg < 2) {
      const float2 sv = stat[l15];
      const int j = 1 + rg;
      f32x4 c = (rg == 0) ? cs[1] : cs[2];
      f32x4 v = (accH - sv.x * c) * sv.y;
      u32x2 pk = {pack2(v[0], v[1]), pack2(v[2], v[3])};
      *(u32x2*)(smem + l15 * QKV_PITCH + ((2 * j + ch) * 16 + 4 * g) * 2) = pk;
    }
  }
  RAW_BARRIER();
  STAMP(0, 10);
  {
    const int half = tid & 1, head = (tid >> 1) & 1, r = tid >> 2;
    const char* base = smem + (r + 16) * QKV_PITCH + (head * 16 + half * 8) * 2;
    const u32x4 qv = *(const u32x4*)base;
    const int grow = panel * 128 + r;
    float lg[8];
#pragma unroll
    for (int wi = 0; wi < 8; ++wi) {
      const u32x4 kv = *(const u32x4*)(base - 2 * wi * QKV_PITCH + 64);
      float d = dot8(qv, kv);
      d += __shfl_xor(d, 1);
      lg[wi] = (grow - 2 * wi >= 0) ? d * 0.25f : -1e9f;
    }
    float mx = lg[0];
#pragma unroll
    for (int wi = 1; wi < 8; ++wi) mx = fmaxf(mx, lg[wi]);
    float sum = 0.f;
#pragma unroll
    for (int wi = 0; wi < 8; ++wi) {
      lg[wi] = __expf(lg[wi] - mx);
      sum += lg[wi];
    }
    const float inv = __builtin_amdgcn_rcpf(sum);
    float ov[8];
#pragma unroll
    for (int d = 0; d < 8; ++d) ov[d] = 0.f;
#pragma unroll
    for (int wi = 0; wi < 8; ++wi) {
      const u32x4 vv = *(const u32x4*)(base - 2 * wi * QKV_PITCH + 128);
      const float p = lg[wi];
#pragma unroll
      for (int d = 0; d < 4; ++d) {
        ov[2 * d] = fmaf(p, bflo(vv[d]), ov[2 * d]);
        ov[2 * d + 1] = fmaf(p, bfhi(vv[d]), ov[2 * d + 1]);
      }
    }
    u32x4 out;
#pragma unroll
    for (int d = 0; d < 4; ++d) out[d] = pack2(ov[2 * d] * inv, ov[2 * d + 1] * inv);
    *(u32x4*)(o + (size_t)grow * D_ + hp * 32 + head * 16 + half * 8) = out;
  }
  asm volatile("" ::"v"(wm0), "v"(wm1), "v"(wm2));
  STAMP(0, 11);
  STAMPR(0, 15);
}

__global__ __launch_bounds__(512, 2) void k1_qkv_attn(const u16* __restrict__ x, const u16* __restrict__ wqkv,
                                                      u16* __restrict__ o, const u16* __restrict__ wo,
                                                      const u16* __restrict__ w1, const u16* __restrict__ w2,
                                                      const u16* __restrict__ outp) {
  extern __shared__ __attribute__((aligned(1024))) char smem[];
  k1_body(x, wqkv, o, wo, w1, w2, outp, smem);
}

static constexpr int G_STAGE = 16 * 1024;
static constexpr int G_R = 8;
static constexpr int G_LDS = G_R * G_STAGE;

template <int K, bool BIAS>
__device__ __forceinline__ void gemm64_body(const u16* __restrict__ A, const u16* __restrict__ W,
                                            const u16* __restrict__ res, const u16* __restrict__ bias,
                                            u16* __restrict__ out, char* smem) {
  constexpr int NS = K / 64;
  constexpr int KID = BIAS ? 3 : 1;
  STAMPR(KID, 14);
  STAMP(KID, 0);
  const int tid = threadIdx.x, lane = tid & 63;
  const int w = __builtin_amdgcn_readfirstlane(tid >> 6);
  const int rg = w & 3, ch = w >> 2;
  const int bid = blockIdx.x;
  const int rt = (bid & 7) * 4 + (bid >> 6), ct = (bid >> 3) & 7;
  const int g = lane >> 4, l15 = lane & 15, q4 = l15 >> 2, p4 = lane & 3;

  const char *srcA, *srcB;
  {
    const int rl = 8 * w + (lane >> 3);
    const int chunk = (lane & 7) ^ ((rl >> 1) & 7);
    srcA = (const char*)(A + (size_t)(rt * 64 + rl) * K + chunk * 8);
    const int b = 4 * w + g;
    const int c4 = b >> 1, pr = b & 1;
    const int chunkb = p4 ^ (2 * ((c4 >> 1) & 1));
    const int krow = 4 * c4 + q4;
    srcB = (const char*)(W + (size_t)krow * D_ + ct * 64 + pr * 32 + chunkb * 8);
  }
  auto issue = [&](int slot) {
    char* base = smem + slot * G_STAGE + w * 1024;
    glds16(srcA, base);
    glds16(srcB, base + 8192);
    srcA += 128;
    srcB += 64 * D_ * 2;
  };
  const int aoff0 = rg * 2048 + l15 * 128 + ((g ^ (l15 >> 1)) * 16);
  const int boff = 8192 + g * 1024 + ch * 256 + q4 * 64 + p4 * 8;
  const int sw = (g & 1) * 32;

  f32x4 acc[2] = {f32x4{0, 0, 0, 0}, f32x4{0, 0, 0, 0}};

#pragma unroll
  for (int s = 0; s < G_R - 1; ++s) issue(s);

  auto compute = [&](int slot) {
    const char* st = smem + slot * G_STAGE;
#pragma unroll
    for (int ks = 0; ks < 2; ++ks) {
      bf16x8 a = lds_a(st + (aoff0 ^ (ks * 64)));
#pragma unroll
      for (int j = 0; j < 2; ++j) {
        const int o2 = boff + ((j * 32) ^ sw);
        bf16x8 b = lds_b(st + o2 + (ks * 8 + 0) * 512, st + o2 + (ks * 8 + 1) * 512);
        acc[j] = mfma16(b, a, acc[j]);
      }
    }
  };

  STAMP(KID, 1);
  constexpr int NMAIN = NS - (G_R - 1);
  int slot = 0;
  for (int s = 0; s < NMAIN; ++s) {
    WAIT_V(2 * (G_R - 2));
    RAW_BARRIER();
    if (s == 0) STAMP(KID, 2);
    issue((slot + G_R - 1) & (G_R - 1));
    compute(slot);
    slot = (slot + 1) & (G_R - 1);
  }
#pragma unroll
  for (int t = 0; t < G_R - 1; ++t) {
    switch (G_R - 2 - t) {
      case 0: WAIT_V(0); break;
      case 1: WAIT_V(2); break;
      case 2: WAIT_V(4); break;
      case 3: WAIT_V(6); break;
      case 4: WAIT_V(8); break;
      case 5: WAIT_V(10); break;
      default: WAIT_V(12); break;
    }
    RAW_BARRIER();
    compute(slot);
    slot = (slot + 1) & (G_R - 1);
  }

  STAMP(KID, 3);
  const int row = rt * 64 + rg * 16 + l15;
#pragma unroll
  for (int j = 0; j < 2; ++j) {
    const int col = ct * 64 + (2 * ch + j) * 16 + 4 * g;
    const u32x2 rv = *(const u32x2*)(res + (size_t)row * D_ + col);
    f32x4 v = acc[j];
    v[0] += bflo(rv[0]);
    v[1] += bfhi(rv[0]);
    v[2] += bflo(rv[1]);
    v[3] += bfhi(rv[1]);
    if (BIAS) {
      const u32x2 bv = *(const u32x2*)(bias + col);
      v[0] += bflo(bv[0]);
      v[1] += bfhi(bv[0]);
      v[2] += bflo(bv[1]);
      v[3] += bfhi(bv[1]);
    }
    u32x2 pk = {pack2(v[0], v[1]), pack2(v[2], v[3])};
    *(u32x2*)(out + (size_t)row * D_ + col) = pk;
  }
  STAMP(KID, 4);
  STAMPR(KID, 15);
}

template <int K, bool BIAS>
__global__ __launch_bounds__(512, 2) void k_gemm64(const u16* __restrict__ A, const u16* __restrict__ W,
                                                   const u16* __restrict__ res, const u16* __restrict__ bias,
                                                   u16* __restrict__ out) {
  extern __shared__ __attribute__((aligned(1024))) char smem[];
  gemm64_body<K, BIAS>(A, W, res, bias, out, smem);
}

static constexpr int K3_STAGE = 32 * 1024;
static constexpr int K3_R = 4;
static constexpr int K3_STAT = K3_R * K3_STAGE;
static constexpr int K3_COLP = K3_STAT + 128 * 8;
static constexpr int K3_LDS = K3_COLP + 4 * 112 * 4;

__device__ __forceinline__ float gelu_tanh(float v) {
  const float u = 0.7978845608028654f * (v + 0.044715f * v * v * v);
  return v * __builtin_amdgcn_rcpf(1.f + __expf(-2.f * u));
}

template <int NT>
__device__ __forceinline__ void k3_body(const u16* __restrict__ b1, u16* __restrict__ h1, char* smem, int rg, int ch,
                                        int lane, int w, int pan, int ct, const char* (&src)[4]) {
  const int g = lane >> 4, l15 = lane & 15, q4 = l15 >> 2, p4 = lane & 3;
  auto issue = [&](int slot) {
    char* base = smem + slot * K3_STAGE + w * 1024;
    glds16(src[0], base);
    glds16(src[1], base + 8192);
    glds16(src[2], base + 16384);
    glds16(src[3], base + 24576);
    src[0] += 128;
    src[1] += 128;
    src[2] += 64 * F_ * 2;
    src[3] += 64 * F_ * 2;
  };
  STAMPR(2, 14);
  STAMP(2, 0);
  const int mtA = 2 * rg + ch, mtB = 2 * rg + 1 - ch;
  const int aoff0 = l15 * 128 + ((g ^ (l15 >> 1)) * 16);
  const int boff = 16384 + g * 2048 + ch * 512 + q4 * 64 + p4 * 8;
  const int sw = (g & 1) * 32;

  f32x4 acc[2][NT], accC[NT], accR = {0, 0, 0, 0};
#pragma unroll
  for (int j = 0; j < NT; ++j) {
    acc[0][j] = f32x4{0, 0, 0, 0};
    acc[1][j] = f32x4{0, 0, 0, 0};
    accC[j] = f32x4{0, 0, 0, 0};
  }
  float ssq = 0.f;
  const bf16x8 ones = ones8();

#pragma unroll
  for (int s = 0; s < K3_R - 1; ++s) issue(s);

#pragma unroll
  for (int s = 0; s < 8; ++s) {
    const int rem = (7 - s) < (K3_R - 2) ? (7 - s) : (K3_R - 2);
    switch (rem) {
      case 0: WAIT_V(0); break;
      case 1: WAIT_V(4); break;
      default: WAIT_V(8); break;
    }
    RAW_BARRIER();
    STAMP(2, 1 + s);
    if (s + K3_R - 1 < 8) issue((s + K3_R - 1) % K3_R);
    const char* st = smem + (s % K3_R) * K3_STAGE;
#pragma unroll
    for (int ks = 0; ks < 2; ++ks) {
      const int ao = aoff0 ^ (ks * 64);
      bf16x8 a0 = lds_a(st + mtA * 2048 + ao);
      bf16x8 a1 = lds_a(st + mtB * 2048 + ao);
      bf16x8 b[NT];
#pragma unroll
      for (int j = 0; j < NT; ++j) {
        const int o2 = boff + (j >> 1) * 256 + (((j & 1) * 32) ^ sw);
        b[j] = lds_b(st + o2 + (ks * 8 + 0) * 1024, st + o2 + (ks * 8 + 1) * 1024);
      }
#pragma unroll
      for (int j = 0; j < NT; ++j) {
        acc[0][j] = mfma16(b[j], a0, acc[0][j]);
        acc[1][j] = mfma16(b[j], a1, acc[1][j]);
      }
      accR = mfma16(ones, a0, accR);
      ssq = sumsq8(a0, ssq);
      if (((s * 2 + ks) & 3) == rg) {
#pragma unroll
        for (int j = 0; j < NT; ++j) accC[j] = mfma16(b[j], ones, accC[j]);
      }
    }
  }
  STAMP(2, 9);
  {
    float2* stat = (float2*)(smem + K3_STAT);
    float* colp = (float*)(smem + K3_COLP);
    ssq += __shfl_xor(ssq, 16);
    ssq += __shfl_xor(ssq, 32);
    if (lane < 16) {
      const float mean = accR[0] * (1.f / D_);
      float var = ssq * (1.f / D_) - mean * mean;
      var = var < 0.f ? 0.f : var;
      const float r1 = rsqrtf(var + EPS);
      const float v1 = var * r1 * r1;
      const float r2 = rsqrtf(v1 + EPS);
      stat[mtA * 16 + lane] = make_float2(mean, r1 * r2);
    }
    if (l15 == 0) {
#pragma unroll
      for (int j = 0; j < NT; ++j) *(f32x4*)(colp + rg * 112 + (4 * ch + j) * 16 + 4 * g) = accC[j];
    }
  }
  RAW_BARRIER();
  {
    const float2* stat = (const float2*)(smem + K3_STAT);
    const float* colp = (const float*)(smem + K3_COLP);
    const float2 sv0 = stat[mtA * 16 + l15], sv1 = stat[mtB * 16 + l15];
#pragma unroll
    for (int j = 0; j < NT; ++j) {
      const int c = (4 * ch + j) * 16 + 4 * g;
      const f32x4 cs = *(const f32x4*)(colp + c) + *(const f32x4*)(colp + 112 + c) + *(const f32x4*)(colp + 224 + c) +
                       *(const f32x4*)(colp + 336 + c);
      const u32x2 bv = *(const u32x2*)(b1 + ct * 112 + c);
      const f32x4 bb = {bflo(bv[0]), bfhi(bv[0]), bflo(bv[1]), bfhi(bv[1])};
#pragma unroll
      for (int i = 0; i < 2; ++i) {
        const float2 sv = i == 0 ? sv0 : sv1;
        const int row = pan * 128 + (i == 0 ? mtA : mtB) * 16 + l15;
        f32x4 v = (acc[i][j] - sv.x * cs) * sv.y + bb;
        u32x2 pk = {pack2(gelu_tanh(v[0]), gelu_tanh(v[1])), pack2(gelu_tanh(v[2]), gelu_tanh(v[3]))};
        *(u32x2*)(h1 + (size_t)row * F_ + ct * 112 + c) = pk;
      }
    }
  }
  STAMP(2, 10);
  STAMPR(2, 15);
}

__global__ __launch_bounds__(512, 2) void k3_mlp1(const u16* __restrict__ h, const u16* __restrict__ w1,
                                                  const u16* __restrict__ b1, u16* __restrict__ h1) {
  extern __shared__ __attribute__((aligned(1024))) char smem[];
  const int tid = threadIdx.x, lane = tid & 63;
  const int w = __builtin_amdgcn_readfirstlane(tid >> 6);
  const int rg = w & 3, ch = w >> 2;
  const int bid = blockIdx.x;
  const int pan = (bid & 7) * 2 + (bid >> 7), ct = (bid >> 3) & 15;
  const int g = lane >> 4, l15 = lane & 15, q4 = l15 >> 2, p4 = lane & 3;
  const char* src[4];
#pragma unroll
  for (int i = 0; i < 2; ++i) {
    const int rl = 8 * (w + 8 * i) + (lane >> 3);
    const int chunk = (lane & 7) ^ ((rl >> 1) & 7);
    src[i] = (const char*)(h + (size_t)(pan * 128 + rl) * D_ + chunk * 8);
  }
#pragma unroll
  for (int i = 0; i < 2; ++i) {
    const int b = 4 * (w + 8 * i) + g;
    const int c4 = b >> 2, pr = b & 3;
    const int chunk = p4 ^ (2 * ((c4 >> 1) & 1));
    int colc = pr * 32 + chunk * 8;
    if (colc >= 112) colc -= 16;
    const int krow = 4 * c4 + q4;
    src[2 + i] = (const char*)(w1 + (size_t)krow * F_ + ct * 112 + colc);
  }
  if (ch == 0)
    k3_body<4>(b1, h1, smem, rg, ch, lane, w, pan, ct, src);
  else
    k3_body<3>(b1, h1, smem, rg, ch, lane, w, pan, ct, src);
}

extern "C" void kernel_launch(void* const* d_in, const int* in_sizes, int n_in, void* d_out, int out_size, void* d_ws,
                              size_t ws_size, hipStream_t stream) {
  const u16* x = (const u16*)d_in[0];
  const u16* wqkv = (const u16*)d_in[1];
  const u16* wo = (const u16*)d_in[2];
  const u16* w1 = (const u16*)d_in[4];
  const u16* b1 = (const u16*)d_in[5];
  const u16* w2 = (const u16*)d_in[6];
  const u16* b2 = (const u16*)d_in[7];
  u16* out = (u16*)d_out;
  char* ws = (char*)d_ws;
  u16* o = (u16*)ws;
  u16* h = (u16*)(ws + (size_t)2 * 1024 * 1024);
  u16* h1 = (u16*)(ws + (size_t)4 * 1024 * 1024);

  static_assert(K1_LDS <= 160 * 1024 && G_LDS <= 160 * 1024 && K3_LDS <= 160 * 1024, "lds");
  (void)hipFuncSetAttribute((const void*)k1_qkv_attn, hipFuncAttributeMaxDynamicSharedMemorySize, K1_LDS);
  (void)hipFuncSetAttribute((const void*)k_gemm64<512, false>, hipFuncAttributeMaxDynamicSharedMemorySize, G_LDS);
  (void)hipFuncSetAttribute((const void*)k3_mlp1, hipFuncAttributeMaxDynamicSharedMemorySize, K3_LDS);
  (void)hipFuncSetAttribute((const void*)k_gemm64<1792, true>, hipFuncAttributeMaxDynamicSharedMemorySize, G_LDS);

  k1_qkv_attn<<<256, 512, K1_LDS, stream>>>(x, wqkv, o, wo, w1, w2, out);
  k_gemm64<512, false><<<256, 512, G_LDS, stream>>>(o, wo, x, b2, h);
  k3_mlp1<<<256, 512, K3_LDS, stream>>>(h, w1, b1, h1);
  k_gemm64<1792, true><<<256, 512, G_LDS, stream>>>(h1, w2, h, b2, out);
}
```

```cpp
#include <hip/hip_runtime.h>
#include <stdint.h>

typedef __attribute__((ext_vector_type(4))) short s16x4;
typedef __attribute__((ext_vector_type(8))) short s16x8;
typedef __attribute__((ext_vector_type(2))) __bf16 bf16x2;
typedef __attribute__((ext_vector_type(8))) __bf16 bf16x8;
typedef __attribute__((ext_vector_type(4))) float f32x4;
typedef __attribute__((ext_vector_type(4))) unsigned u32x4;
typedef __attribute__((ext_vector_type(2))) unsigned u32x2;
typedef unsigned short u16;
#define LDS_AS __attribute__((address_space(3)))

#define WAIT_V(n) asm volatile("s_waitcnt vmcnt(%0)" ::"n"(n) : "memory")
#define WAIT_L0() asm volatile("s_waitcnt lgkmcnt(0)" ::: "memory")
#define RAW_BARRIER()                  \
  do {                                 \
    WAIT_L0();                         \
    __builtin_amdgcn_s_barrier();      \
    asm volatile("" ::: "memory");     \
  } while (0)

#define STAMP(kid, i)
#define STAMPR(kid, i)
static constexpr int S_ = 2048;
static constexpr int D_ = 512;
static constexpr int F_ = 1792;
static constexpr float EPS = 1e-5f;

__device__ __forceinline__ unsigned pack2(float a, float b) {
  bf16x2 v = {(__bf16)a, (__bf16)b};
  return __builtin_bit_cast(unsigned, v);
}
__device__ __forceinline__ float bflo(unsigned u) { return __uint_as_float(u << 16); }
__device__ __forceinline__ float bfhi(unsigned u) { return __uint_as_float(u & 0xffff0000u); }

__device__ __forceinline__ f32x4 mfma16(bf16x8 a, bf16x8 b, f32x4 c) {
  return __builtin_amdgcn_mfma_f32_16x16x32_bf16(a, b, c, 0, 0, 0);
}
__device__ __forceinline__ bf16x8 lds_a(const char* p) { return *(const bf16x8*)p; }
__device__ __forceinline__ bf16x8 lds_b(const char* p0, const char* p1) {
  s16x4 a = __builtin_amdgcn_ds_read_tr16_b64_v4i16((LDS_AS s16x4*)p0);
  s16x4 b = __builtin_amdgcn_ds_read_tr16_b64_v4i16((LDS_AS s16x4*)p1);
  s16x8 f = __builtin_shufflevector(a, b, 0, 1, 2, 3, 4, 5, 6, 7);
  return __builtin_bit_cast(bf16x8, f);
}
__device__ __forceinline__ float sumsq8(bf16x8 a, float c) {
  bf16x2 p0 = __builtin_shufflevector(a, a, 0, 1);
  bf16x2 p1 = __builtin_shufflevector(a, a, 2, 3);
  bf16x2 p2 = __builtin_shufflevector(a, a, 4, 5);
  bf16x2 p3 = __builtin_shufflevector(a, a, 6, 7);
  c = __builtin_amdgcn_fdot2_f32_bf16(p0, p0, c, false);
  c = __builtin_amdgcn_fdot2_f32_bf16(p1, p1, c, false);
  c = __builtin_amdgcn_fdot2_f32_bf16(p2, p2, c, false);
  c = __builtin_amdgcn_fdot2_f32_bf16(p3, p3, c, false);
  return c;
}
__device__ __forceinline__ float dot2u(unsigned a, unsigned b, float c) {
  return __builtin_amdgcn_fdot2_f32_bf16(__builtin_bit_cast(bf16x2, a), __builtin_bit_cast(bf16x2, b), c, false);
}
__device__ __forceinline__ float dot8(u32x4 a, u32x4 b) {
  const unsigned a0 = a[0], a1 = a[1], a2 = a[2], a3 = a[3];
  const unsigned b0 = b[0], b1 = b[1], b2 = b[2], b3 = b[3];
  float c = 0.f;
  c = dot2u(a0, b0, c);
  c = dot2u(a1, b1, c);
  c = dot2u(a2, b2, c);
  c = dot2u(a3, b3, c);
  return c;
}
__device__ __forceinline__ bf16x8 ones8() {
  s16x8 o = {0x3F80, 0x3F80, 0x3F80, 0x3F80, 0x3F80, 0x3F80, 0x3F80, 0x3F80};
  return __builtin_bit_cast(bf16x8, o);
}
__device__ __forceinline__ void glds16(const void* g, char* l) {
  __builtin_amdgcn_global_load_lds((const unsigned*)g, (unsigned*)l, 16, 0, 0);
}

static constexpr int K1_STAGE = 30 * 1024;
static constexpr int K1_R = 4;
static constexpr int K1_ABYTES = 18 * 1024;
static constexpr int K1_STAT = K1_R * K1_STAGE;
static constexpr int K1_COLP = K1_STAT + 144 * 8;
static constexpr int K1_LDS = K1_COLP + 4 * 96 * 4;
static constexpr int QKV_PITCH = 208;

__device__ __forceinline__ void k1_body(const u16* __restrict__ x, const u16* __restrict__ wqkv, u16* __restrict__ o,
                                        const u16* __restrict__ wo, const u16* __restrict__ w1,
                                        const u16* __restrict__ w2, const u16* __restrict__ outp, char* smem) {
  const int tid = threadIdx.x, lane = tid & 63;
  const int w = __builtin_amdgcn_readfirstlane(tid >> 6);
  const int rg = w >> 1, ch = w & 1;
  const int bid = blockIdx.x;
  const int panel = (bid & 7) * 2 + (bid >> 7), hp = (bid >> 3) & 15;
  const int row0 = panel * 128 - 16;
  STAMPR(0, 14);
  STAMP(0, 0);
  const int g = lane >> 4, l15 = lane & 15, q4 = l15 >> 2, p4 = lane & 3;

  unsigned cw0 = 0, cw1 = 0, cw2 = 0, cw3 = 0, cw4 = 0, cw5 = 0, cw6 = 0;
  if (w == 7) {
    uint64_t a0, a1, a2, a3, a4;
#define SYMADDR(dst, sym)                                                                                   \
  asm volatile("s_getpc_b64 s[20:21]\n s_add_u32 s20, s20, " sym "@rel32@lo+4\n s_addc_u32 s21, s21, " sym    \
               "@rel32@hi+12\n s_mov_b64 %0, s[20:21]"                                                        \
               : "=s"(dst)::"scc", "s20", "s21")
    SYMADDR(a0, "_Z11k1_qkv_attnPKtS0_PtS0_S0_S0_S0_");
    SYMADDR(a1, "_Z7k3_mlp1PKtS0_S0_Pt");
    SYMADDR(a2, "_Z8k_gemm64ILi512ELb0EEvPKtS1_S1_S1_Pt");
    SYMADDR(a3, "_Z8k_gemm64ILi1792ELb1EEvPKtS1_S1_S1_Pt");
    SYMADDR(a4, "_Z7k3_mlp1PKtS0_S0_Pt.kd");
    typedef const __attribute__((address_space(1))) unsigned* gp;
    const int lo = lane * 128;
    cw0 = *(gp)(a0 + lo);
    cw1 = *(gp)(a0 + min(lo + 8192, 11776));
    cw2 = *(gp)(a1 + lo);
    cw3 = *(gp)(a1 + lo + 8192);
    cw4 = *(gp)(a2 + min(lo, 2304));
    cw5 = *(gp)(a3 + min(lo, 6144));
    cw6 = *(gp)(a4);
  }

  const char* src[4];
  int stride[4];
#pragma unroll
  for (int i = 0; i < 4; ++i) {
    const int p = w + 8 * i;
    if (p < 18) {
      const int rl = 8 * p + (lane >> 3);
      const int chunk = (lane & 7) ^ ((rl >> 1) & 7);
      int gr = row0 + rl;
      gr = gr < 0 ? 0 : gr;
      src[i] = (const char*)(x + (size_t)gr * D_ + chunk * 8);
      stride[i] = 128;
    } else {
      const int pb = (p < 30 ? p : 29) - 18;
      const int b = 4 * pb + g;
      const int c4 = b / 3, pr = b - 3 * c4;
      const int chunk = p4 ^ (2 * ((c4 >> 1) & 1));
      const int krow = 4 * c4 + q4;
      src[i] = (const char*)(wqkv + (size_t)krow * (3 * D_) + pr * D_ + hp * 32 + chunk * 8);
      stride[i] = 64 * 3 * D_ * 2;
    }
  }
  const bool has4 = (w < 6);
  auto issue = [&](int slot) {
    char* base = smem + slot * K1_STAGE + w * 1024;
#pragma unroll
    for (int i = 0; i < 3; ++i) {
      glds16(src[i], base + i * 8192);
      src[i] += stride[i];
    }
    if (has4) {
      glds16(src[3], base + 3 * 8192);
      src[3] += stride[3];
    }
  };

  const int mtA = 1 + 2 * rg + ch, mtB = 2 + 2 * rg - ch;
  const int aoff0 = l15 * 128 + ((g ^ (l15 >> 1)) * 16);
  const int boff = K1_ABYTES + g * 1536 + q4 * 64 + ((ch ^ (g & 1)) * 32) + p4 * 8;

  f32x4 acc[2][3], accH = {0, 0, 0, 0}, accR = {0, 0, 0, 0}, accRH = {0, 0, 0, 0}, accC[3];
#pragma unroll
  for (int j = 0; j < 3; ++j) {
    acc[0][j] = f32x4{0, 0, 0, 0};
    acc[1][j] = f32x4{0, 0, 0, 0};
    accC[j] = f32x4{0, 0, 0, 0};
  }
  float ssq = 0.f, ssqH = 0.f;
  const bf16x8 ones = ones8();

#pragma unroll
  for (int s = 0; s < K1_R - 1; ++s) issue(s);

#pragma unroll
  for (int s = 0; s < 8; ++s) {
    constexpr int dummy = 0;
    (void)dummy;
    const int rem = (7 - s) < (K1_R - 2) ? (7 - s) : (K1_R - 2);
    if (has4) {
      switch (rem) {
        case 0: WAIT_V(0); break;
        case 1: WAIT_V(4); break;
        default: WAIT_V(8); break;
      }
    } else {
      switch (rem) {
        case 0: WAIT_V(0); break;
        case 1: WAIT_V(3); break;
        default: WAIT_V(6); break;
      }
    }
    RAW_BARRIER();
    STAMP(0, 1 + s);
    if (s + K1_R - 1 < 8) issue((s + K1_R - 1) % K1_R);
    const char* st = smem + (s % K1_R) * K1_STAGE;
#pragma unroll
    for (int ks = 0; ks < 2; ++ks) {
      const int ao = aoff0 ^ (ks * 64);
      bf16x8 a0 = lds_a(st + mtA * 2048 + ao);
      bf16x8 a1 = lds_a(st + mtB * 2048 + ao);
      bf16x8 b[3];
#pragma unroll
      for (int j = 0; j < 3; ++j)
        b[j] = lds_b(st + boff + (ks * 8 + 0) * 768 + j * 256, st + boff + (ks * 8 + 1) * 768 + j * 256);
#pragma unroll
      for (int j = 0; j < 3; ++j) {
        acc[0][j] = mfma16(b[j], a0, acc[0][j]);
        acc[1][j] = mfma16(b[j], a1, acc[1][j]);
      }
      accR = mfma16(ones, a0, accR);
      ssq = sumsq8(a0, ssq);
      if (rg < 2) {
        bf16x8 ah = lds_a(st + ao);
        accH = mfma16(rg == 0 ? b[1] : b[2], ah, accH);
        if (w == 0) {
          accRH = mfma16(ones, ah, accRH);
          ssqH = sumsq8(ah, ssqH);
        }
      }
      if (((s * 2 + ks) & 3) == rg) {
#pragma unroll
        for (int j = 0; j < 3; ++j) accC[j] = mfma16(b[j], ones, accC[j]);
      }
    }
  }

  STAMP(0, 9);
  u32x4 wm0, wm1, wm2;
  {
    const int t16 = (bid * 512 + tid) * 16;
    const char* p0 = (t16 < 1792 * 1024) ? (const char*)w1 + t16 : (const char*)wo + (t16 - 1792 * 1024) ;
    const char* p1 = (t16 < 1792 * 1024) ? (const char*)w2 + t16 : (const char*)wo + (t16 - 1792 * 1024) + 256 * 1024;
    wm0 = *(const u32x4*)p0;
    wm1 = *(const u32x4*)p1;
    const int j = (bid >> 3) * 8 + (tid & 7);
    const char* p2 = (j < 176) ? (const char*)o + (size_t)(2 * 1024 * 1024) + (size_t)j * 65536 : (const char*)outp + (size_t)(j - 176) * 16384;
    wm2 = u32x4{0, 0, 0, 0};
    if (tid < 8) wm2 = *(const u32x4*)p2;
  }
  {
    float2* stat = (float2*)(smem + K1_STAT);
    float* colp = (float*)(smem + K1_COLP);
    ssq += __shfl_xor(ssq, 16);
    ssq += __shfl_xor(ssq, 32);
    if (lane < 16) {
      const float mean = accR[0] * (1.f / D_);
      float var = ssq * (1.f / D_) - mean * mean;
      var = var < 0.f ? 0.f : var;
      stat[mtA * 16 + lane] = make_float2(mean, rsqrtf(var + EPS));
    }
    if (w == 0) {
      ssqH += __shfl_xor(ssqH, 16);
      ssqH += __shfl_xor(ssqH, 32);
      if (lane < 16) {
        const float mean = accRH[0] * (1.f / D_);
        float var = ssqH * (1.f / D_) - mean * mean;
        var = var < 0.f ? 0.f : var;
        stat[lane] = make_float2(mean, rsqrtf(var + EPS));
      }
    }
    if (l15 == 0) {
#pragma unroll
      for (int j = 0; j < 3; ++j) *(f32x4*)(colp + rg * 96 + (2 * j + ch) * 16 + 4 * g) = accC[j];
    }
  }
  RAW_BARRIER();
  {
    const float2* stat = (const float2*)(smem + K1_STAT);
    const float* colp = (const float*)(smem + K1_COLP);
    f32x4 cs[3];
#pragma unroll
    for (int j = 0; j < 3; ++j) {
      const int c = (2 * j + ch) * 16 + 4 * g;
      cs[j] = *(const f32x4*)(colp + c) + *(const f32x4*)(colp + 96 + c) + *(const f32x4*)(colp + 192 + c) +
              *(const f32x4*)(colp + 288 + c);
    }
#pragma unroll
    for (int i = 0; i < 2; ++i) {
      const int rl = (i == 0 ? mtA : mtB) * 16 + l15;
      const float2 sv = stat[rl];
#pragma unroll
      for (int j = 0; j < 3; ++j) {
        f32x4 v = (acc[i][j] - sv.x * cs[j]) * sv.y;
        u32x2 pk = {pack2(v[0], v[1]), pack2(v[2], v[3])};
        *(u32x2*)(smem + rl * QKV_PITCH + ((2 * j + ch) * 16 + 4 * g) * 2) = pk;
      }
    }
    if (rg < 2) {
      const float2 sv = stat[l15];
      const int j = 1 + rg;
      f32x4 c = (rg == 0) ? cs[1] : cs[2];
      f32x4 v = (accH - sv.x * c) * sv.y;
      u32x2 pk = {pack2(v[0], v[1]), pack2(v[2], v[3])};
      *(u32x2*)(smem + l15 * QKV_PITCH + ((2 * j + ch) * 16 + 4 * g) * 2) = pk;
    }
  }
  RAW_BARRIER();
  STAMP(0, 10);
  {
    const int half = tid & 1, head = (tid >> 1) & 1, r = tid >> 2;
    const char* base = smem + (r + 16) * QKV_PITCH + (head * 16 + half * 8) * 2;
    const u32x4 qv = *(const u32x4*)base;
    const int grow = panel * 128 + r;
    float lg[8];
#pragma unroll
    for (int wi = 0; wi < 8; ++wi) {
      const u32x4 kv = *(const u32x4*)(base - 2 * wi * QKV_PITCH + 64);
      float d = dot8(qv, kv);
      d += __shfl_xor(d, 1);
      lg[wi] = (grow - 2 * wi >= 0) ? d * 0.25f : -1e9f;
    }
    float mx = lg[0];
#pragma unroll
    for (int wi = 1; wi < 8; ++wi) mx = fmaxf(mx, lg[wi]);
    float sum = 0.f;
#pragma unroll
    for (int wi = 0; wi < 8; ++wi) {
      lg[wi] = __expf(lg[wi] - mx);
      sum += lg[wi];
    }
    const float inv = __builtin_amdgcn_rcpf(sum);
    float ov[8];
#pragma unroll
    for (int d = 0; d < 8; ++d) ov[d] = 0.f;
#pragma unroll
    for (int wi = 0; wi < 8; ++wi) {
      const u32x4 vv = *(const u32x4*)(base - 2 * wi * QKV_PITCH + 128);
      const float p = lg[wi];
#pragma unroll
      for (int d = 0; d < 4; ++d) {
        ov[2 * d] = fmaf(p, bflo(vv[d]), ov[2 * d]);
        ov[2 * d + 1] = fmaf(p, bfhi(vv[d]), ov[2 * d + 1]);
      }
    }
    u32x4 out;
#pragma unroll
    for (int d = 0; d < 4; ++d) out[d] = pack2(ov[2 * d] * inv, ov[2 * d + 1] * inv);
    *(u32x4*)(o + (size_t)grow * D_ + hp * 32 + head * 16 + half * 8) = out;
  }
  asm volatile("" ::"v"(wm0), "v"(wm1), "v"(wm2), "v"(cw0), "v"(cw1), "v"(cw2), "v"(cw3), "v"(cw4), "v"(cw5), "v"(cw6));
  STAMP(0, 11);
  STAMPR(0, 15);
}

__global__ __launch_bounds__(512, 2) void k1_qkv_attn(const u16* __restrict__ x, const u16* __restrict__ wqkv,
                                                      u16* __restrict__ o, const u16* __restrict__ wo,
                                                      const u16* __restrict__ w1, const u16* __restrict__ w2,
                                                      const u16* __restrict__ outp) {
  extern __shared__ __attribute__((aligned(1024))) char smem[];
  k1_body(x, wqkv, o, wo, w1, w2, outp, smem);
}

static constexpr int G_STAGE = 16 * 1024;
static constexpr int G_R = 8;
static constexpr int G_LDS = G_R * G_STAGE;

template <int K, bool BIAS>
__device__ __forceinline__ void gemm64_body(const u16* __restrict__ A, const u16* __restrict__ W,
                                            const u16* __restrict__ res, const u16* __restrict__ bias,
                                            u16* __restrict__ out, char* smem) {
  constexpr int NS = K / 64;
  constexpr int KID = BIAS ? 3 : 1;
  STAMPR(KID, 14);
  STAMP(KID, 0);
  const int tid = threadIdx.x, lane = tid & 63;
  const int w = __builtin_amdgcn_readfirstlane(tid >> 6);
  const int rg = w & 3, ch = w >> 2;
  const int bid = blockIdx.x;
  const int rt = (bid & 7) * 4 + (bid >> 6), ct = (bid >> 3) & 7;
  const int g = lane >> 4, l15 = lane & 15, q4 = l15 >> 2, p4 = lane & 3;

  const char *srcA, *srcB;
  {
    const int rl = 8 * w + (lane >> 3);
    const int chunk = (lane & 7) ^ ((rl >> 1) & 7);
    srcA = (const char*)(A + (size_t)(rt * 64 + rl) * K + chunk * 8);
    const int b = 4 * w + g;
    const int c4 = b >> 1, pr = b & 1;
    const int chunkb = p4 ^ (2 * ((c4 >> 1) & 1));
    const int krow = 4 * c4 + q4;
    srcB = (const char*)(W + (size_t)krow * D_ + ct * 64 + pr * 32 + chunkb * 8);
  }
  auto issue = [&](int slot) {
    char* base = smem + slot * G_STAGE + w * 1024;
    glds16(srcA, base);
    glds16(srcB, base + 8192);
    srcA += 128;
    srcB += 64 * D_ * 2;
  };
  const int aoff0 = rg * 2048 + l15 * 128 + ((g ^ (l15 >> 1)) * 16);
  const int boff = 8192 + g * 1024 + ch * 256 + q4 * 64 + p4 * 8;
  const int sw = (g & 1) * 32;

  f32x4 acc[2] = {f32x4{0, 0, 0, 0}, f32x4{0, 0, 0, 0}};

#pragma unroll
  for (int s = 0; s < G_R - 1; ++s) issue(s);

  auto compute = [&](int slot) {
    const char* st = smem + slot * G_STAGE;
#pragma unroll
    for (int ks = 0; ks < 2; ++ks) {
      bf16x8 a = lds_a(st + (aoff0 ^ (ks * 64)));
#pragma unroll
      for (int j = 0; j < 2; ++j) {
        const int o2 = boff + ((j * 32) ^ sw);
        bf16x8 b = lds_b(st + o2 + (ks * 8 + 0) * 512, st + o2 + (ks * 8 + 1) * 512);
        acc[j] = mfma16(b, a, acc[j]);
      }
    }
  };

  STAMP(KID, 1);
  constexpr int NMAIN = NS - (G_R - 1);
  int slot = 0;
  for (int s = 0; s < NMAIN; ++s) {
    WAIT_V(2 * (G_R - 2));
    RAW_BARRIER();
    if (s == 0) STAMP(KID, 2);
    issue((slot + G_R - 1) & (G_R - 1));
    compute(slot);
    slot = (slot + 1) & (G_R - 1);
  }
#pragma unroll
  for (int t = 0; t < G_R - 1; ++t) {
    switch (G_R - 2 - t) {
      case 0: WAIT_V(0); break;
      case 1: WAIT_V(2); break;
      case 2: WAIT_V(4); break;
      case 3: WAIT_V(6); break;
      case 4: WAIT_V(8); break;
      case 5: WAIT_V(10); break;
      default: WAIT_V(12); break;
    }
    RAW_BARRIER();
    compute(slot);
    slot = (slot + 1) & (G_R - 1);
  }

  STAMP(KID, 3);
  const int row = rt * 64 + rg * 16 + l15;
#pragma unroll
  for (int j = 0; j < 2; ++j) {
    const int col = ct * 64 + (2 * ch + j) * 16 + 4 * g;
    const u32x2 rv = *(const u32x2*)(res + (size_t)row * D_ + col);
    f32x4 v = acc[j];
    v[0] += bflo(rv[0]);
    v[1] += bfhi(rv[0]);
    v[2] += bflo(rv[1]);
    v[3] += bfhi(rv[1]);
    if (BIAS) {
      const u32x2 bv = *(const u32x2*)(bias + col);
      v[0] += bflo(bv[0]);
      v[1] += bfhi(bv[0]);
      v[2] += bflo(bv[1]);
      v[3] += bfhi(bv[1]);
    }
    u32x2 pk = {pack2(v[0], v[1]), pack2(v[2], v[3])};
    *(u32x2*)(out + (size_t)row * D_ + col) = pk;
  }
  STAMP(KID, 4);
  STAMPR(KID, 15);
}

template <int K, bool BIAS>
__global__ __launch_bounds__(512, 2) void k_gemm64(const u16* __restrict__ A, const u16* __restrict__ W,
                                                   const u16* __restrict__ res, const u16* __restrict__ bias,
                                                   u16* __restrict__ out) {
  extern __shared__ __attribute__((aligned(1024))) char smem[];
  gemm64_body<K, BIAS>(A, W, res, bias, out, smem);
}

static constexpr int K3_STAGE = 32 * 1024;
static constexpr int K3_R = 4;
static constexpr int K3_STAT = K3_R * K3_STAGE;
static constexpr int K3_COLP = K3_STAT + 128 * 8;
static constexpr int K3_LDS = K3_COLP + 4 * 112 * 4;

__device__ __forceinline__ float gelu_tanh(float v) {
  const float u = 0.7978845608028654f * (v + 0.044715f * v * v * v);
  return v * __builtin_amdgcn_rcpf(1.f + __expf(-2.f * u));
}

template <int NT>
__device__ __forceinline__ void k3_body(const u16* __restrict__ b1, u16* __restrict__ h1, char* smem, int rg, int ch,
                                        int lane, int w, int pan, int ct, const char* (&src)[4]) {
  const int g = lane >> 4, l15 = lane & 15, q4 = l15 >> 2, p4 = lane & 3;
  auto issue = [&](int slot) {
    char* base = smem + slot * K3_STAGE + w * 1024;
    glds16(src[0], base);
    glds16(src[1], base + 8192);
    glds16(src[2], base + 16384);
    glds16(src[3], base + 24576);
    src[0] += 128;
    src[1] += 128;
    src[2] += 64 * F_ * 2;
    src[3] += 64 * F_ * 2;
  };
  STAMPR(2, 14);
  STAMP(2, 0);
  const int mtA = 2 * rg + ch, mtB = 2 * rg + 1 - ch;
  const int aoff0 = l15 * 128 + ((g ^ (l15 >> 1)) * 16);
  const int boff = 16384 + g * 2048 + ch * 512 + q4 * 64 + p4 * 8;
  const int sw = (g & 1) * 32;

  f32x4 acc[2][NT], accC[NT], accR = {0, 0, 0, 0};
#pragma unroll
  for (int j = 0; j < NT; ++j) {
    acc[0][j] = f32x4{0, 0, 0, 0};
    acc[1][j] = f32x4{0, 0, 0, 0};
    accC[j] = f32x4{0, 0, 0, 0};
  }
  float ssq = 0.f;
  const bf16x8 ones = ones8();

#pragma unroll
  for (int s = 0; s < K3_R - 1; ++s) issue(s);

#pragma unroll
  for (int s = 0; s < 8; ++s) {
    const int rem = (7 - s) < (K3_R - 2) ? (7 - s) : (K3_R - 2);
    switch (rem) {
      case 0: WAIT_V(0); break;
      case 1: WAIT_V(4); break;
      default: WAIT_V(8); break;
    }
    RAW_BARRIER();
    STAMP(2, 1 + s);
    if (s + K3_R - 1 < 8) issue((s + K3_R - 1) % K3_R);
    const char* st = smem + (s % K3_R) * K3_STAGE;
#pragma unroll
    for (int ks = 0; ks < 2; ++ks) {
      const int ao = aoff0 ^ (ks * 64);
      bf16x8 a0 = lds_a(st + mtA * 2048 + ao);
      bf16x8 a1 = lds_a(st + mtB * 2048 + ao);
      bf16x8 b[NT];
#pragma unroll
      for (int j = 0; j < NT; ++j) {
        const int o2 = boff + (j >> 1) * 256 + (((j & 1) * 32) ^ sw);
        b[j] = lds_b(st + o2 + (ks * 8 + 0) * 1024, st + o2 + (ks * 8 + 1) * 1024);
      }
#pragma unroll
      for (int j = 0; j < NT; ++j) {
        acc[0][j] = mfma16(b[j], a0, acc[0][j]);
        acc[1][j] = mfma16(b[j], a1, acc[1][j]);
      }
      accR = mfma16(ones, a0, accR);
      ssq = sumsq8(a0, ssq);
      if (((s * 2 + ks) & 3) == rg) {
#pragma unroll
        for (int j = 0; j < NT; ++j) accC[j] = mfma16(b[j], ones, accC[j]);
      }
    }
  }
  STAMP(2, 9);
  {
    float2* stat = (float2*)(smem + K3_STAT);
    float* colp = (float*)(smem + K3_COLP);
    ssq += __shfl_xor(ssq, 16);
    ssq += __shfl_xor(ssq, 32);
    if (lane < 16) {
      const float mean = accR[0] * (1.f / D_);
      float var = ssq * (1.f / D_) - mean * mean;
      var = var < 0.f ? 0.f : var;
      const float r1 = rsqrtf(var + EPS);
      const float v1 = var * r1 * r1;
      const float r2 = rsqrtf(v1 + EPS);
      stat[mtA * 16 + lane] = make_float2(mean, r1 * r2);
    }
    if (l15 == 0) {
#pragma unroll
      for (int j = 0; j < NT; ++j) *(f32x4*)(colp + rg * 112 + (4 * ch + j) * 16 + 4 * g) = accC[j];
    }
  }
  RAW_BARRIER();
  {
    const float2* stat = (const float2*)(smem + K3_STAT);
    const float* colp = (const float*)(smem + K3_COLP);
    const float2 sv0 = stat[mtA * 16 + l15], sv1 = stat[mtB * 16 + l15];
#pragma unroll
    for (int j = 0; j < NT; ++j) {
      const int c = (4 * ch + j) * 16 + 4 * g;
      const f32x4 cs = *(const f32x4*)(colp + c) + *(const f32x4*)(colp + 112 + c) + *(const f32x4*)(colp + 224 + c) +
                       *(const f32x4*)(colp + 336 + c);
      const u32x2 bv = *(const u32x2*)(b1 + ct * 112 + c);
      const f32x4 bb = {bflo(bv[0]), bfhi(bv[0]), bflo(bv[1]), bfhi(bv[1])};
#pragma unroll
      for (int i = 0; i < 2; ++i) {
        const float2 sv = i == 0 ? sv0 : sv1;
        const int row = pan * 128 + (i == 0 ? mtA : mtB) * 16 + l15;
        f32x4 v = (acc[i][j] - sv.x * cs) * sv.y + bb;
        u32x2 pk = {pack2(gelu_tanh(v[0]), gelu_tanh(v[1])), pack2(gelu_tanh(v[2]), gelu_tanh(v[3]))};
        *(u32x2*)(h1 + (size_t)row * F_ + ct * 112 + c) = pk;
      }
    }
  }
  STAMP(2, 10);
  STAMPR(2, 15);
}

__global__ __launch_bounds__(512, 2) void k3_mlp1(const u16* __restrict__ h, const u16* __restrict__ w1,
                                                  const u16* __restrict__ b1, u16* __restrict__ h1) {
  extern __shared__ __attribute__((aligned(1024))) char smem[];
  const int tid = threadIdx.x, lane = tid & 63;
  const int w = __builtin_amdgcn_readfirstlane(tid >> 6);
  const int rg = w & 3, ch = w >> 2;
  const int bid = blockIdx.x;
  const int pan = (bid & 7) * 2 + (bid >> 7), ct = (bid >> 3) & 15;
  const int g = lane >> 4, l15 = lane & 15, q4 = l15 >> 2, p4 = lane & 3;
  const char* src[4];
#pragma unroll
  for (int i = 0; i < 2; ++i) {
    const int rl = 8 * (w + 8 * i) + (lane >> 3);
    const int chunk = (lane & 7) ^ ((rl >> 1) & 7);
    src[i] = (const char*)(h + (size_t)(pan * 128 + rl) * D_ + chunk * 8);
  }
#pragma unroll
  for (int i = 0; i < 2; ++i) {
    const int b = 4 * (w + 8 * i) + g;
    const int c4 = b >> 2, pr = b & 3;
    const int chunk = p4 ^ (2 * ((c4 >> 1) & 1));
    int colc = pr * 32 + chunk * 8;
    if (colc >= 112) colc -= 16;
    const int krow = 4 * c4 + q4;
    src[2 + i] = (const char*)(w1 + (size_t)krow * F_ + ct * 112 + colc);
  }
  if (ch == 0)
    k3_body<4>(b1, h1, smem, rg, ch, lane, w, pan, ct, src);
  else
    k3_body<3>(b1, h1, smem, rg, ch, lane, w, pan, ct, src);
}

extern "C" void kernel_launch(void* const* d_in, const int* in_sizes, int n_in, void* d_out, int out_size, void* d_ws,
                              size_t ws_size, hipStream_t stream) {
  const u16* x = (const u16*)d_in[0];
  const u16* wqkv = (const u16*)d_in[1];
  const u16* wo = (const u16*)d_in[2];
  const u16* w1 = (const u16*)d_in[4];
  const u16* b1 = (const u16*)d_in[5];
  const u16* w2 = (const u16*)d_in[6];
  const u16* b2 = (const u16*)d_in[7];
  u16* out = (u16*)d_out;
  char* ws = (char*)d_ws;
  u16* o = (u16*)ws;
  u16* h = (u16*)(ws + (size_t)2 * 1024 * 1024);
  u16* h1 = (u16*)(ws + (size_t)4 * 1024 * 1024);

  static_assert(K1_LDS <= 160 * 1024 && G_LDS <= 160 * 1024 && K3_LDS <= 160 * 1024, "lds");
  (void)hipFuncSetAttribute((const void*)k1_qkv_attn, hipFuncAttributeMaxDynamicSharedMemorySize, K1_LDS);
  (void)hipFuncSetAttribute((const void*)k_gemm64<512, false>, hipFuncAttributeMaxDynamicSharedMemorySize, G_LDS);
  (void)hipFuncSetAttribute((const void*)k3_mlp1, hipFuncAttributeMaxDynamicSharedMemorySize, K3_LDS);
  (void)hipFuncSetAttribute((const void*)k_gemm64<1792, true>, hipFuncAttributeMaxDynamicSharedMemorySize, G_LDS);

  k1_qkv_attn<<<256, 512, K1_LDS, stream>>>(x, wqkv, o, wo, w1, w2, out);
  k_gemm64<512, false><<<256, 512, G_LDS, stream>>>(o, wo, x, b2, h);
  k3_mlp1<<<256, 512, K3_LDS, stream>>>(h, w1, b1, h1);
  k_gemm64<1792, true><<<256, 512, G_LDS, stream>>>(h1, w2, h, b2, out);
}
```

```cpp
#include <hip/hip_runtime.h>
#include <stdint.h>

typedef __attribute__((ext_vector_type(4))) short s16x4;
typedef __attribute__((ext_vector_type(8))) short s16x8;
typedef __attribute__((ext_vector_type(2))) __bf16 bf16x2;
typedef __attribute__((ext_vector_type(8))) __bf16 bf16x8;
typedef __attribute__((ext_vector_type(4))) float f32x4;
typedef __attribute__((ext_vector_type(4))) unsigned u32x4;
typedef __attribute__((ext_vector_type(2))) unsigned u32x2;
typedef unsigned short u16;
#define LDS_AS __attribute__((address_space(3)))

#define WAIT_V(n) asm volatile("s_waitcnt vmcnt(%0)" ::"n"(n) : "memory")
#define WAIT_L0() asm volatile("s_waitcnt lgkmcnt(0)" ::: "memory")
#define RAW_BARRIER()                  \
  do {                                 \
    WAIT_L0();                         \
    __builtin_amdgcn_s_barrier();      \
    asm volatile("" ::: "memory");     \
  } while (0)

#define SCHED_PIN() __builtin_amdgcn_sched_barrier(0)
#define STAMP(kid, i)
#define STAMPR(kid, i)
static constexpr int S_ = 2048;
static constexpr int D_ = 512;
static constexpr int F_ = 1792;
static constexpr float EPS = 1e-5f;

__device__ __forceinline__ unsigned pack2(float a, float b) {
  bf16x2 v = {(__bf16)a, (__bf16)b};
  return __builtin_bit_cast(unsigned, v);
}
__device__ __forceinline__ float bflo(unsigned u) { return __uint_as_float(u << 16); }
__device__ __forceinline__ float bfhi(unsigned u) { return __uint_as_float(u & 0xffff0000u); }

__device__ __forceinline__ f32x4 mfma16(bf16x8 a, bf16x8 b, f32x4 c) {
  return __builtin_amdgcn_mfma_f32_16x16x32_bf16(a, b, c, 0, 0, 0);
}
__device__ __forceinline__ bf16x8 lds_a(const char* p) { return *(const bf16x8*)p; }
__device__ __forceinline__ bf16x8 lds_b(const char* p0, const char* p1) {
  s16x4 a = __builtin_amdgcn_ds_read_tr16_b64_v4i16((LDS_AS s16x4*)p0);
  s16x4 b = __builtin_amdgcn_ds_read_tr16_b64_v4i16((LDS_AS s16x4*)p1);
  s16x8 f = __builtin_shufflevector(a, b, 0, 1, 2, 3, 4, 5, 6, 7);
  return __builtin_bit_cast(bf16x8, f);
}
__device__ __forceinline__ float sumsq8(bf16x8 a, float c) {
  bf16x2 p0 = __builtin_shufflevector(a, a, 0, 1);
  bf16x2 p1 = __builtin_shufflevector(a, a, 2, 3);
  bf16x2 p2 = __builtin_shufflevector(a, a, 4, 5);
  bf16x2 p3 = __builtin_shufflevector(a, a, 6, 7);
  c = __builtin_amdgcn_fdot2_f32_bf16(p0, p0, c, false);
  c = __builtin_amdgcn_fdot2_f32_bf16(p1, p1, c, false);
  c = __builtin_amdgcn_fdot2_f32_bf16(p2, p2, c, false);
  c = __builtin_amdgcn_fdot2_f32_bf16(p3, p3, c, false);
  return c;
}
__device__ __forceinline__ float dot2u(unsigned a, unsigned b, float c) {
  return __builtin_amdgcn_fdot2_f32_bf16(__builtin_bit_cast(bf16x2, a), __builtin_bit_cast(bf16x2, b), c, false);
}
__device__ __forceinline__ float dot8(u32x4 a, u32x4 b) {
  const unsigned a0 = a[0], a1 = a[1], a2 = a[2], a3 = a[3];
  const unsigned b0 = b[0], b1 = b[1], b2 = b[2], b3 = b[3];
  float c = 0.f;
  c = dot2u(a0, b0, c);
  c = dot2u(a1, b1, c);
  c = dot2u(a2, b2, c);
  c = dot2u(a3, b3, c);
  return c;
}
__device__ __forceinline__ bf16x8 ones8() {
  s16x8 o = {0x3F80, 0x3F80, 0x3F80, 0x3F80, 0x3F80, 0x3F80, 0x3F80, 0x3F80};
  return __builtin_bit_cast(bf16x8, o);
}
__device__ __forceinline__ void glds16(const void* g, char* l) {
  __builtin_amdgcn_global_load_lds((const unsigned*)g, (unsigned*)l, 16, 0, 0);
}

#define ST_AUX 0
__device__ __forceinline__ void st16(void* base, unsigned off, u32x4 v) {
  __amdgpu_buffer_rsrc_t r = __builtin_amdgcn_make_buffer_rsrc(base, 0, 0x7fffffff, 0x00020000);
  __builtin_amdgcn_raw_buffer_store_b128(v, r, off, 0, ST_AUX);
}
__device__ __forceinline__ void swap16(unsigned& a, unsigned& b) {
  auto r = __builtin_amdgcn_permlane16_swap(a, b, false, false);
  a = r[0];
  b = r[1];
}

static constexpr int K1_STAGE = 30 * 1024;
static constexpr int K1_R = 4;
static constexpr int K1_ABYTES = 18 * 1024;
static constexpr int K1_STAT = K1_R * K1_STAGE;
static constexpr int K1_COLP = K1_STAT + 144 * 8;
static constexpr int K1_HPART = K1_COLP + 4 * 96 * 4;
static constexpr int K1_LDS = K1_HPART + 4 * 16 * 8;
static constexpr int QKV_PITCH = 208;

__device__ __forceinline__ void k1_body(const u16* __restrict__ x, const u16* __restrict__ wqkv, u16* __restrict__ o,
                                        const u16* __restrict__ wo, const u16* __restrict__ w1,
                                        const u16* __restrict__ w2, const u16* __restrict__ outp, char* smem) {
  const int tid = threadIdx.x, lane = tid & 63;
  const int w = __builtin_amdgcn_readfirstlane(tid >> 6);
  const int rg = w >> 1, ch = w & 1;
  const int bid = blockIdx.x;
  const int panel = (bid & 7) * 2 + (bid >> 7), hp = (bid >> 3) & 15;
  const int row0 = panel * 128 - 16;
  STAMPR(0, 14);
  STAMP(0, 0);
  const int g = lane >> 4, l15 = lane & 15, q4 = l15 >> 2, p4 = lane & 3;

  const char* src[4];
  int stride[4];
#pragma unroll
  for (int i = 0; i < 4; ++i) {
    const int p = w + 8 * i;
    if (p < 18) {
      const int rl = 8 * p + (lane >> 3);
      const int chunk = (lane & 7) ^ ((rl >> 1) & 7);
      int gr = row0 + rl;
      gr = gr < 0 ? 0 : gr;
      src[i] = (const char*)(x + (size_t)gr * D_ + chunk * 8);
      stride[i] = 128;
    } else {
      const int pb = (p < 30 ? p : 29) - 18;
      const int b = 4 * pb + g;
      const int c4 = b / 3, pr = b - 3 * c4;
      const int chunk = p4 ^ (2 * ((c4 >> 1) & 1));
      const int krow = 4 * c4 + q4;
      src[i] = (const char*)(wqkv + (size_t)krow * (3 * D_) + pr * D_ + hp * 32 + chunk * 8);
      stride[i] = 64 * 3 * D_ * 2;
    }
  }
  const bool has4 = (w < 6);
  auto issue = [&](int slot) {
    char* base = smem + slot * K1_STAGE + w * 1024;
#pragma unroll
    for (int i = 0; i < 3; ++i) {
      glds16(src[i], base + i * 8192);
      src[i] += stride[i];
    }
    if (has4) {
      glds16(src[3], base + 3 * 8192);
      src[3] += stride[3];
    }
  };

  const int mtA = 1 + 2 * rg + ch, mtB = 2 + 2 * rg - ch;
  const int aoff0 = l15 * 128 + ((g ^ (l15 >> 1)) * 16);
  const int boff = K1_ABYTES + g * 1536 + q4 * 64 + ((ch ^ (g & 1)) * 32) + p4 * 8;

  f32x4 acc[2][3], accH = {0, 0, 0, 0}, accR = {0, 0, 0, 0}, accRH = {0, 0, 0, 0}, accC[3];
#pragma unroll
  for (int j = 0; j < 3; ++j) {
    acc[0][j] = f32x4{0, 0, 0, 0};
    acc[1][j] = f32x4{0, 0, 0, 0};
    accC[j] = f32x4{0, 0, 0, 0};
  }
  float ssq = 0.f, ssqH = 0.f;
  const bf16x8 ones = ones8();
  const bool odd = (rg & 1) != 0;

  struct Frag {
    bf16x8 a0, a1, ah, b[3];
  };
  auto rd = [&](Frag& f, const char* st, int ks) {
    const int ao = aoff0 ^ (ks * 64);
    f.a0 = lds_a(st + mtA * 2048 + ao);
#pragma unroll
    for (int j = 0; j < 3; ++j)
      f.b[j] = lds_b(st + boff + (ks * 8 + 0) * 768 + j * 256, st + boff + (ks * 8 + 1) * 768 + j * 256);
    f.a1 = lds_a(st + mtB * 2048 + ao);
    f.ah = lds_a(st + ao);
  };
  auto mm = [&](const Frag& f, int s) {
#pragma unroll
    for (int j = 0; j < 3; ++j) acc[0][j] = mfma16(f.b[j], f.a0, acc[0][j]);
    accR = mfma16(ones, f.a0, accR);
#pragma unroll
    for (int j = 0; j < 3; ++j) acc[1][j] = mfma16(f.b[j], f.a1, acc[1][j]);
    ssq = sumsq8(f.a0, ssq);
    const bf16x8 bh = odd ? f.b[2] : f.b[1];
    accH = mfma16(bh, f.ah, accH);
    if ((s & 3) == rg) {
#pragma unroll
      for (int j = 0; j < 3; ++j) accC[j] = mfma16(f.b[j], ones, accC[j]);
      accRH = mfma16(ones, f.ah, accRH);
      ssqH = sumsq8(f.ah, ssqH);
    }
  };

#pragma unroll
  for (int s = 0; s < K1_R - 1; ++s) issue(s);

  Frag fx, fy;
#pragma unroll
  for (int s = 0; s < 8; ++s) {
    const int rem = (7 - s) < (K1_R - 2) ? (7 - s) : (K1_R - 2);
    if (has4) {
      switch (rem) {
        case 0: WAIT_V(0); break;
        case 1: WAIT_V(4); break;
        default: WAIT_V(8); break;
      }
    } else {
      switch (rem) {
        case 0: WAIT_V(0); break;
        case 1: WAIT_V(3); break;
        default: WAIT_V(6); break;
      }
    }
    RAW_BARRIER();
    STAMP(0, 1 + s);
    const char* st = smem + (s % K1_R) * K1_STAGE;
    rd(fx, st, 0);
    SCHED_PIN();
    if (s > 0) mm(fy, s - 1);
    SCHED_PIN();
    asm volatile("" ::"v"(fx.a0), "v"(fx.a1), "v"(fx.ah), "v"(fx.b[0]), "v"(fx.b[1]), "v"(fx.b[2]));
    rd(fy, st, 1);
    SCHED_PIN();
    mm(fx, s);
    SCHED_PIN();
    if (s + K1_R - 1 < 8) issue((s + K1_R - 1) % K1_R);
  }
  mm(fy, 7);

  STAMP(0, 9);
  u32x4 wm0, wm1, wm2;
  {
    const int t16 = (bid * 512 + tid) * 16;
    const char* p0 = (t16 < 1792 * 1024) ? (const char*)w1 + t16 : (const char*)wo + (t16 - 1792 * 1024) ;
    const char* p1 = (t16 < 1792 * 1024) ? (const char*)w2 + t16 : (const char*)wo + (t16 - 1792 * 1024) + 256 * 1024;
    wm0 = *(const u32x4*)p0;
    wm1 = *(const u32x4*)p1;
    const int j = (bid >> 3) * 8 + (tid & 7);
    const char* p2 = (j < 176) ? (const char*)o + (size_t)(2 * 1024 * 1024) + (size_t)j * 65536 : (const char*)outp + (size_t)(j - 176) * 16384;
    wm2 = u32x4{0, 0, 0, 0};
    if (tid < 8) wm2 = *(const u32x4*)p2;
  }
  {
    float2* stat = (float2*)(smem + K1_STAT);
    float* colp = (float*)(smem + K1_COLP);
    float2* hpart = (float2*)(smem + K1_HPART);
    ssq += __shfl_xor(ssq, 16);
    ssq += __shfl_xor(ssq, 32);
    if (lane < 16) {
      const float mean = accR[0] * (1.f / D_);
      float var = ssq * (1.f / D_) - mean * mean;
      var = var < 0.f ? 0.f : var;
      stat[mtA * 16 + lane] = make_float2(mean, rsqrtf(var + EPS));
    }
    if (ch == 0) {
      ssqH += __shfl_xor(ssqH, 16);
      ssqH += __shfl_xor(ssqH, 32);
      if (lane < 16) hpart[rg * 16 + lane] = make_float2(accRH[0], ssqH);
    }
    if (l15 == 0) {
#pragma unroll
      for (int j = 0; j < 3; ++j) *(f32x4*)(colp + rg * 96 + (2 * j + ch) * 16 + 4 * g) = accC[j];
    }
  }
  RAW_BARRIER();
  {
    const float2* stat = (const float2*)(smem + K1_STAT);
    const float* colp = (const float*)(smem + K1_COLP);
    f32x4 cs[3];
#pragma unroll
    for (int j = 0; j < 3; ++j) {
      const int c = (2 * j + ch) * 16 + 4 * g;
      cs[j] = *(const f32x4*)(colp + c) + *(const f32x4*)(colp + 96 + c) + *(const f32x4*)(colp + 192 + c) +
              *(const f32x4*)(colp + 288 + c);
    }
#pragma unroll
    for (int i = 0; i < 2; ++i) {
      const int rl = (i == 0 ? mtA : mtB) * 16 + l15;
      const float2 sv = stat[rl];
#pragma unroll
      for (int j = 0; j < 3; ++j) {
        f32x4 v = (acc[i][j] - sv.x * cs[j]) * sv.y;
        u32x2 pk = {pack2(v[0], v[1]), pack2(v[2], v[3])};
        *(u32x2*)(smem + rl * QKV_PITCH + ((2 * j + ch) * 16 + 4 * g) * 2) = pk;
      }
    }
    if (rg < 2) {
      const float2* hpart = (const float2*)(smem + K1_HPART);
      const float2 h0 = hpart[l15], h1 = hpart[16 + l15], h2 = hpart[32 + l15], h3 = hpart[48 + l15];
      float2 sv;
      sv.x = ((h0.x + h1.x) + (h2.x + h3.x)) * (1.f / D_);
      float var = ((h0.y + h1.y) + (h2.y + h3.y)) * (1.f / D_) - sv.x * sv.x;
      var = var < 0.f ? 0.f : var;
      sv.y = rsqrtf(var + EPS);
      const int j = 1 + rg;
      f32x4 c = (rg == 0) ? cs[1] : cs[2];
      f32x4 v = (accH - sv.x * c) * sv.y;
      u32x2 pk = {pack2(v[0], v[1]), pack2(v[2], v[3])};
      *(u32x2*)(smem + l15 * QKV_PITCH + ((2 * j + ch) * 16 + 4 * g) * 2) = pk;
    }
  }
  RAW_BARRIER();
  STAMP(0, 10);
  {
    const int half = tid & 1, head = (tid >> 1) & 1, r = tid >> 2;
    const char* base = smem + (r + 16) * QKV_PITCH + (head * 16 + half * 8) * 2;
    const u32x4 qv = *(const u32x4*)base;
    const int grow = panel * 128 + r;
    float lg[8];
#pragma unroll
    for (int wi = 0; wi < 8; ++wi) {
      const u32x4 kv = *(const u32x4*)(base - 2 * wi * QKV_PITCH + 64);
      float d = dot8(qv, kv);
      d += __shfl_xor(d, 1);
      lg[wi] = (grow - 2 * wi >= 0) ? d * 0.25f : -1e9f;
    }
    float mx = lg[0];
#pragma unroll
    for (int wi = 1; wi < 8; ++wi) mx = fmaxf(mx, lg[wi]);
    float sum = 0.f;
#pragma unroll
    for (int wi = 0; wi < 8; ++wi) {
      lg[wi] = __expf(lg[wi] - mx);
      sum += lg[wi];
    }
    const float inv = __builtin_amdgcn_rcpf(sum);
    float ov[8];
#pragma unroll
    for (int d = 0; d < 8; ++d) ov[d] = 0.f;
#pragma unroll
    for (int wi = 0; wi < 8; ++wi) {
      const u32x4 vv = *(const u32x4*)(base - 2 * wi * QKV_PITCH + 128);
      const float p = lg[wi];
#pragma unroll
      for (int d = 0; d < 4; ++d) {
        ov[2 * d] = fmaf(p, bflo(vv[d]), ov[2 * d]);
        ov[2 * d + 1] = fmaf(p, bfhi(vv[d]), ov[2 * d + 1]);
      }
    }
    u32x4 out;
#pragma unroll
    for (int d = 0; d < 4; ++d) out[d] = pack2(ov[2 * d] * inv, ov[2 * d + 1] * inv);
    *(u32x4*)(o + (size_t)grow * D_ + hp * 32 + head * 16 + half * 8) = out;
  }
  asm volatile("" ::"v"(wm0), "v"(wm1), "v"(wm2));
  STAMP(0, 11);
  STAMPR(0, 15);
}

__global__ __launch_bounds__(512, 2) void k1_qkv_attn(const u16* __restrict__ x, const u16* __restrict__ wqkv,
                                                      u16* __restrict__ o, const u16* __restrict__ wo,
                                                      const u16* __restrict__ w1, const u16* __restrict__ w2,
                                                      const u16* __restrict__ outp) {
  extern __shared__ __attribute__((aligned(1024))) char smem[];
  k1_body(x, wqkv, o, wo, w1, w2, outp, smem);
}

static constexpr int G_STAGE = 16 * 1024;
static constexpr int G_R = 8;
static constexpr int G_LDS = G_R * G_STAGE;

template <int K, bool BIAS>
__device__ __forceinline__ void gemm64_body(const u16* __restrict__ A, const u16* __restrict__ W,
                                            const u16* __restrict__ res, const u16* __restrict__ bias,
                                            u16* __restrict__ out, float* __restrict__ stats, char* smem) {
  constexpr int NS = K / 64;
  constexpr int KID = BIAS ? 3 : 1;
  STAMPR(KID, 14);
  STAMP(KID, 0);
  const int tid = threadIdx.x, lane = tid & 63;
  const int w = __builtin_amdgcn_readfirstlane(tid >> 6);
  const int rg = w & 3, ch = w >> 2;
  const int bid = blockIdx.x;
  const int rt = (bid & 7) * 4 + (bid >> 6), ct = (bid >> 3) & 7;
  const int g = lane >> 4, l15 = lane & 15, q4 = l15 >> 2, p4 = lane & 3;

  const int row = rt * 64 + rg * 16 + l15;
  const int col8 = ct * 64 + ch * 32 + (g & 1) * 16 + (g >> 1) * 8;
  u32x4 rv = *(const u32x4*)(res + (size_t)row * D_ + col8);
  u32x4 bv = {0, 0, 0, 0};
  if (BIAS) bv = *(const u32x4*)(bias + col8);

  const char *srcA, *srcB;
  {
    const int rl = 8 * w + (lane >> 3);
    const int chunk = (lane & 7) ^ ((rl >> 1) & 7);
    srcA = (const char*)(A + (size_t)(rt * 64 + rl) * K + chunk * 8);
    const int b = 4 * w + g;
    const int c4 = b >> 1, pr = b & 1;
    const int chunkb = p4 ^ (2 * ((c4 >> 1) & 1));
    const int krow = 4 * c4 + q4;
    srcB = (const char*)(W + (size_t)krow * D_ + ct * 64 + pr * 32 + chunkb * 8);
  }
  auto issue = [&](int slot) {
    char* base = smem + slot * G_STAGE + w * 1024;
    glds16(srcA, base);
    glds16(srcB, base + 8192);
    srcA += 128;
    srcB += 64 * D_ * 2;
  };
  const int aoff0 = rg * 2048 + l15 * 128 + ((g ^ (l15 >> 1)) * 16);
  const int boff = 8192 + g * 1024 + ch * 256 + q4 * 64 + p4 * 8;
  const int sw = (g & 1) * 32;

  f32x4 acc[2] = {f32x4{0, 0, 0, 0}, f32x4{0, 0, 0, 0}};

#pragma unroll
  for (int s = 0; s < G_R - 1; ++s) issue(s);

  auto compute = [&](int slot) {
    const char* st = smem + slot * G_STAGE;
#pragma unroll
    for (int ks = 0; ks < 2; ++ks) {
      bf16x8 a = lds_a(st + (aoff0 ^ (ks * 64)));
#pragma unroll
      for (int j = 0; j < 2; ++j) {
        const int o2 = boff + ((j * 32) ^ sw);
        bf16x8 b = lds_b(st + o2 + (ks * 8 + 0) * 512, st + o2 + (ks * 8 + 1) * 512);
        acc[j] = mfma16(b, a, acc[j]);
      }
    }
  };

  STAMP(KID, 1);
  constexpr int NMAIN = NS - (G_R - 1);
  int slot = 0;
  for (int s = 0; s < NMAIN; ++s) {
    WAIT_V(2 * (G_R - 2));
    RAW_BARRIER();
    if (s == 0) STAMP(KID, 2);
    issue((slot + G_R - 1) & (G_R - 1));
    compute(slot);
    slot = (slot + 1) & (G_R - 1);
  }
#pragma unroll
  for (int t = 0; t < G_R - 1; ++t) {
    switch (G_R - 2 - t) {
      case 0: WAIT_V(0); break;
      case 1: WAIT_V(2); break;
      case 2: WAIT_V(4); break;
      case 3: WAIT_V(6); break;
      case 4: WAIT_V(8); break;
      case 5: WAIT_V(10); break;
      default: WAIT_V(12); break;
    }
    RAW_BARRIER();
    compute(slot);
    slot = (slot + 1) & (G_R - 1);
  }

  STAMP(KID, 3);
  {
    unsigned r0 = rv[0], r1 = rv[1], r2 = rv[2], r3 = rv[3];
    swap16(r0, r2);
    swap16(r1, r3);
    f32x4 v0 = acc[0], v1 = acc[1];
    v0[0] += bflo(r0);
    v0[1] += bfhi(r0);
    v0[2] += bflo(r1);
    v0[3] += bfhi(r1);
    v1[0] += bflo(r2);
    v1[1] += bfhi(r2);
    v1[2] += bflo(r3);
    v1[3] += bfhi(r3);
    if (BIAS) {
      unsigned b0 = bv[0], b1 = bv[1], b2 = bv[2], b3 = bv[3];
      swap16(b0, b2);
      swap16(b1, b3);
      v0[0] += bflo(b0);
      v0[1] += bfhi(b0);
      v0[2] += bflo(b1);
      v0[3] += bfhi(b1);
      v1[0] += bflo(b2);
      v1[1] += bfhi(b2);
      v1[2] += bflo(b3);
      v1[3] += bfhi(b3);
    }
    unsigned p0 = pack2(v0[0], v0[1]), p1 = pack2(v0[2], v0[3]);
    unsigned p2 = pack2(v1[0], v1[1]), p3 = pack2(v1[2], v1[3]);
    if (!BIAS) {
      float sm = dot2u(p0, 0x3F803F80u, 0.f), sq = dot2u(p0, p0, 0.f);
      sm = dot2u(p1, 0x3F803F80u, sm);
      sq = dot2u(p1, p1, sq);
      sm = dot2u(p2, 0x3F803F80u, sm);
      sq = dot2u(p2, p2, sq);
      sm = dot2u(p3, 0x3F803F80u, sm);
      sq = dot2u(p3, p3, sq);
      sm += __shfl_xor(sm, 16);
      sq += __shfl_xor(sq, 16);
      sm += __shfl_xor(sm, 32);
      sq += __shfl_xor(sq, 32);
      if (g == 0) *(float2*)(stats + (size_t)row * 32 + (ct * 2 + ch) * 2) = make_float2(sm, sq);
    }
    swap16(p0, p2);
    swap16(p1, p3);
    *(u32x4*)(out + (size_t)row * D_ + col8) = u32x4{p0, p1, p2, p3};
  }
  STAMP(KID, 4);
  STAMPR(KID, 15);
}

template <int K, bool BIAS>
__global__ __launch_bounds__(512, 2) void k_gemm64(const u16* __restrict__ A, const u16* __restrict__ W,
                                                   const u16* __restrict__ res, const u16* __restrict__ bias,
                                                   u16* __restrict__ out, float* __restrict__ stats) {
  extern __shared__ __attribute__((aligned(1024))) char smem[];
  gemm64_body<K, BIAS>(A, W, res, bias, out, stats, smem);
}

static constexpr int M1_RING = 65536;
static constexpr int M1_XCH = M1_RING + 5 * 16384;
static constexpr int K3_LDS = M1_XCH + 2048;

__device__ __forceinline__ float gelu_tanh(float v) {
  const float p = fmaf(v * v, -2.f * 1.4426950408889634f * 0.7978845608028654f * 0.044715f,
                       -2.f * 1.4426950408889634f * 0.7978845608028654f);
  const float e = __builtin_amdgcn_exp2f(v * p);
  return v * __builtin_amdgcn_rcpf(1.f + e);
}
__device__ __forceinline__ f32x4 ld16_async(const void* p) {
  f32x4 r;
  asm volatile("global_load_dwordx4 %0, %1, off" : "=v"(r) : "v"(p) : "memory");
  return r;
}
__device__ __forceinline__ u32x2 ld8_async(const void* p) {
  u32x2 r;
  asm volatile("global_load_dwordx2 %0, %1, off" : "=v"(r) : "v"(p) : "memory");
  return r;
}

__device__ __forceinline__ void mlp1_body(const u16* __restrict__ h, const u16* __restrict__ w1,
                                          const u16* __restrict__ b1, const float* __restrict__ stats,
                                          u16* __restrict__ h1, char* smem) {
  const int tid = threadIdx.x, lane = tid & 63;
  const int w = __builtin_amdgcn_readfirstlane(tid >> 6);
  const int cg = w & 3, rgp = w >> 2;
  const int bid = blockIdx.x;
  const int xcd = bid & 7, idx = bid >> 3;
  const int pan = (xcd >> 1) * 4 + (idx & 3), ct = (xcd & 1) * 7 + (idx >> 2);
  const int R0 = pan * 128, C0 = ct * 128;
  const int g = lane >> 4, l15 = lane & 15, q4 = l15 >> 2, p4 = lane & 3;
  STAMPR(2, 14);
  STAMP(2, 0);

  f32x4 sp[2];
#pragma unroll
  for (int i = 0; i < 2; ++i) sp[i] = ld16_async(stats + (size_t)(R0 + 16 * w + 8 * i + (lane >> 3)) * 32 + (lane & 7) * 4);
  u32x2 bv[2];
#pragma unroll
  for (int j = 0; j < 2; ++j) bv[j] = ld8_async(b1 + C0 + cg * 32 + j * 16 + 4 * g);

  const char* srcK[2];
  const char* srcA[2];
  const char* srcB[2];
#pragma unroll
  for (int r = 0; r < 2; ++r) {
    {
      const int rl = 8 * w + 4 * r + g;
      const int chunk = l15 ^ (rl & 15);
      srcK[r] = (const char*)(h + (size_t)(R0 + rl) * D_ + chunk * 8);
    }
    {
      const int rl = 2 * w + r;
      const int chunk = (lane & 48) | ((lane & 15) ^ rl);
      srcA[r] = (const char*)(h + (size_t)(R0 + 64 + rl) * D_ + chunk * 8);
    }
    const int b = 4 * (w + 8 * r) + g;
    const int c4 = b >> 2, pr = b & 3;
    const int chunkb = p4 ^ (2 * ((c4 >> 1) & 1));
    srcB[r] = (const char*)(w1 + (size_t)(4 * c4 + q4) * F_ + C0 + pr * 32 + chunkb * 8);
  }
  auto issueK = [&](char* dst) {
    glds16(srcK[0], dst + w * 2048);
    glds16(srcK[1], dst + w * 2048 + 1024);
    srcK[0] += 256;
    srcK[1] += 256;
  };
  auto issueA = [&](char* dst) {
    glds16(srcA[0], dst + w * 2048);
    glds16(srcA[1], dst + w * 2048 + 1024);
    srcA[0] += 16 * D_ * 2;
    srcA[1] += 16 * D_ * 2;
  };
  auto issueB = [&](char* dst) {
    glds16(srcB[0], dst + w * 1024);
    glds16(srcB[1], dst + w * 1024 + 8192);
    srcB[0] += 64 * F_ * 2;
    srcB[1] += 64 * F_ * 2;
  };
  char* ring = smem + M1_RING;
  issueK(smem);
  issueB(ring);
  issueB(ring + 16384);
  issueK(smem + 16384);

  int aoff[4], koff[4];
#pragma unroll
  for (int a = 0; a < 4; ++a) {
    aoff[a] = l15 * 1024 + ((g ^ l15 ^ (4 * a)) * 16);
    koff[a] = rgp * 4096 + l15 * 256 + ((g ^ l15 ^ (4 * a)) * 16);
  }
  const int boff = g * 2048 + cg * 256 + q4 * 64 + p4 * 8;
  const int sw = (g & 1) * 32;

  asm volatile("s_waitcnt vmcnt(8)" : "+v"(sp[0]), "+v"(sp[1]), "+v"(bv[0]), "+v"(bv[1]) : : "memory");
  float2* rstat = (float2*)(smem + M1_XCH + 1024);
#pragma unroll
  for (int i = 0; i < 2; ++i) {
    float sm = sp[i][0] + sp[i][2];
    float sq = sp[i][1] + sp[i][3];
#pragma unroll
    for (int m = 1; m < 8; m <<= 1) {
      sm += __shfl_xor(sm, m);
      sq += __shfl_xor(sq, m);
    }
    const float mean = sm * (1.f / D_);
    float var = sq * (1.f / D_) - mean * mean;
    var = var < 0.f ? 0.f : var;
    const float r1 = rsqrtf(var + EPS);
    const float v1 = var * r1 * r1;
    const float r2 = rsqrtf(v1 + EPS);
    const float rs = r1 * r2;
    if ((lane & 7) == 0) rstat[16 * w + 8 * i + (lane >> 3)] = make_float2(rs, -mean * rs);
  }

  bf16x8 bf[16][2];
  f32x4 acc[4][2], accC[2];
#pragma unroll
  for (int t = 0; t < 4; ++t) {
    acc[t][0] = f32x4{0, 0, 0, 0};
    acc[t][1] = f32x4{0, 0, 0, 0};
  }
  accC[0] = f32x4{0, 0, 0, 0};
  accC[1] = f32x4{0, 0, 0, 0};
  const bf16x8 ones = ones8();
  STAMP(2, 1);

#pragma unroll
  for (int s = 0; s < 8; ++s) {
    if (s & 1) WAIT_V(6); else WAIT_V(4);
    RAW_BARRIER();
    if (s == 0) STAMP(2, 2);
    const char* st = ring + (s % 5) * 16384;
#pragma unroll
    for (int ks = 0; ks < 2; ++ks) {
      const int kk = 2 * s + ks;
#pragma unroll
      for (int j = 0; j < 2; ++j) {
        const int o2 = boff + ((j * 32) ^ sw);
        bf[kk][j] = lds_b(st + o2 + (ks * 8 + 0) * 1024, st + o2 + (ks * 8 + 1) * 1024);
      }
      const char* ap = smem + (kk >> 2) * 16384 + koff[kk & 3];
      const bf16x8 a0 = lds_a(ap), a1 = lds_a(ap + 8192);
#pragma unroll
      for (int j = 0; j < 2; ++j) {
        acc[0][j] = mfma16(bf[kk][j], a0, acc[0][j]);
        acc[1][j] = mfma16(bf[kk][j], a1, acc[1][j]);
      }
    }
#pragma unroll
    for (int j = 0; j < 2; ++j) {
      const bf16x8 bs = rgp ? bf[2 * s + 1][j] : bf[2 * s][j];
      accC[j] = mfma16(bs, ones, accC[j]);
    }
    switch (s) {
      case 0: issueB(ring + 2 * 16384); issueB(ring + 3 * 16384); break;
      case 1: issueK(smem + 2 * 16384); break;
      case 2: issueB(ring + 4 * 16384); issueB(ring); break;
      case 3: issueK(smem + 3 * 16384); break;
      case 4: issueB(ring + 16384); issueB(ring + 2 * 16384); break;
      case 5: issueA(ring + 3 * 16384); break;
      case 6: issueA(ring + 4 * 16384); issueA(ring); break;
      default: issueA(ring + 16384); break;
    }
  }
  STAMP(2, 3);
  float* xch = (float*)(smem + M1_XCH);
  if (l15 == 0) {
    *(f32x4*)(xch + w * 32 + 4 * g) = accC[0];
    *(f32x4*)(xch + w * 32 + 16 + 4 * g) = accC[1];
  }
  WAIT_V(4);
  RAW_BARRIER();
  f32x4 tb[2];
  f32x4 cs[2];
#pragma unroll
  for (int j = 0; j < 2; ++j) {
    cs[j] = accC[j] + *(const f32x4*)(xch + (w ^ 4) * 32 + j * 16 + 4 * g);
    tb[j] = f32x4{bflo(bv[j][0]), bfhi(bv[j][0]), bflo(bv[j][1]), bfhi(bv[j][1])};
  }
  auto epi = [&](int t) {
    const float2 rm = rstat[(rgp + 2 * t) * 16 + l15];
    unsigned pk[2][2];
#pragma unroll
    for (int j = 0; j < 2; ++j) {
      float o4[4];
#pragma unroll
      for (int i = 0; i < 4; ++i) {
        const float tt = fmaf(rm.y, cs[j][i], tb[j][i]);
        o4[i] = gelu_tanh(fmaf(acc[t][j][i], rm.x, tt));
      }
      pk[j][0] = pack2(o4[0], o4[1]);
      pk[j][1] = pack2(o4[2], o4[3]);
    }
    swap16(pk[0][0], pk[1][0]);
    swap16(pk[0][1], pk[1][1]);
    const int row = R0 + (rgp + 2 * t) * 16 + l15;
    st16(h1, (unsigned)(row * F_ + C0 + cg * 32 + (g & 1) * 16 + (g >> 1) * 8) * 2u,
         u32x4{pk[0][0], pk[0][1], pk[1][0], pk[1][1]});
  };
  auto mm = [&](int t, const char* base) {
#pragma unroll
    for (int kk = 0; kk < 16; ++kk) {
      const bf16x8 a = lds_a(base + aoff[kk & 3] + (kk >> 2) * 256);
      acc[t][0] = mfma16(bf[kk][0], a, acc[t][0]);
      acc[t][1] = mfma16(bf[kk][1], a, acc[t][1]);
    }
  };
  if (rgp == 0) {
    epi(0);
    mm(2, ring + 3 * 16384);
    epi(1);
    WAIT_V(2);
    RAW_BARRIER();
    STAMP(2, 4);
    mm(3, ring + 0 * 16384);
    epi(2);
    epi(3);
  } else {
    mm(2, ring + 4 * 16384);
    epi(0);
    WAIT_V(1);
    RAW_BARRIER();
    epi(1);
    mm(3, ring + 1 * 16384);
    epi(2);
    epi(3);
  }
  STAMP(2, 5);
  STAMPR(2, 15);
}

__global__ __launch_bounds__(512, 2) void k3_mlp1(const u16* __restrict__ h, const u16* __restrict__ w1,
                                                  const u16* __restrict__ b1, const float* __restrict__ stats,
                                                  u16* __restrict__ h1) {
  extern __shared__ __attribute__((aligned(1024))) char smem[];
  mlp1_body(h, w1, b1, stats, h1, smem);
}

extern "C" void kernel_launch(void* const* d_in, const int* in_sizes, int n_in, void* d_out, int out_size, void* d_ws,
                              size_t ws_size, hipStream_t stream) {
  const u16* x = (const u16*)d_in[0];
  const u16* wqkv = (const u16*)d_in[1];
  const u16* wo = (const u16*)d_in[2];
  const u16* w1 = (const u16*)d_in[4];
  const u16* b1 = (const u16*)d_in[5];
  const u16* w2 = (const u16*)d_in[6];
  const u16* b2 = (const u16*)d_in[7];
  u16* out = (u16*)d_out;
  char* ws = (char*)d_ws;
  u16* o = (u16*)ws;
  u16* h = (u16*)(ws + (size_t)2 * 1024 * 1024);
  u16* h1 = (u16*)(ws + (size_t)4 * 1024 * 1024);
  float* stats = (float*)(ws + (size_t)12 * 1024 * 1024);

  static_assert(K1_LDS <= 160 * 1024 && G_LDS <= 160 * 1024 && K3_LDS <= 160 * 1024, "lds");
  (void)hipFuncSetAttribute((const void*)k1_qkv_attn, hipFuncAttributeMaxDynamicSharedMemorySize, K1_LDS);
  (void)hipFuncSetAttribute((const void*)k_gemm64<512, false>, hipFuncAttributeMaxDynamicSharedMemorySize, G_LDS);
  (void)hipFuncSetAttribute((const void*)k3_mlp1, hipFuncAttributeMaxDynamicSharedMemorySize, K3_LDS);
  (void)hipFuncSetAttribute((const void*)k_gemm64<1792, true>, hipFuncAttributeMaxDynamicSharedMemorySize, G_LDS);

  k1_qkv_attn<<<256, 512, K1_LDS, stream>>>(x, wqkv, o, wo, w1, w2, out);
  k_gemm64<512, false><<<256, 512, G_LDS, stream>>>(o, wo, x, b2, h, stats);
  k3_mlp1<<<224, 512, K3_LDS, stream>>>(h, w1, b1, stats, h1);
  k_gemm64<1792, true><<<256, 512, G_LDS, stream>>>(h1, w2, h, b2, out, stats);
}
```

```cpp
#include <hip/hip_runtime.h>
#include <stdint.h>

typedef __attribute__((ext_vector_type(4))) short s16x4;
typedef __attribute__((ext_vector_type(8))) short s16x8;
typedef __attribute__((ext_vector_type(2))) __bf16 bf16x2;
typedef __attribute__((ext_vector_type(8))) __bf16 bf16x8;
typedef __attribute__((ext_vector_type(4))) float f32x4;
typedef __attribute__((ext_vector_type(4))) unsigned u32x4;
typedef __attribute__((ext_vector_type(2))) unsigned u32x2;
typedef unsigned short u16;
#define LDS_AS __attribute__((address_space(3)))

#define WAIT_V(n) asm volatile("s_waitcnt vmcnt(%0)" ::"n"(n) : "memory")
#define WAIT_L0() asm volatile("s_waitcnt lgkmcnt(0)" ::: "memory")
#define RAW_BARRIER()                  \
  do {                                 \
    WAIT_L0();                         \
    __builtin_amdgcn_s_barrier();      \
    asm volatile("" ::: "memory");     \
  } while (0)

#define SCHED_PIN() __builtin_amdgcn_sched_barrier(0)
#define STAMP(kid, i)
#define STAMPR(kid, i)
static constexpr int S_ = 2048;
static constexpr int D_ = 512;
static constexpr int F_ = 1792;
static constexpr float EPS = 1e-5f;

__device__ __forceinline__ unsigned pack2(float a, float b) {
  bf16x2 v = {(__bf16)a, (__bf16)b};
  return __builtin_bit_cast(unsigned, v);
}
__device__ __forceinline__ float bflo(unsigned u) { return __uint_as_float(u << 16); }
__device__ __forceinline__ float bfhi(unsigned u) { return __uint_as_float(u & 0xffff0000u); }

__device__ __forceinline__ f32x4 mfma16(bf16x8 a, bf16x8 b, f32x4 c) {
  return __builtin_amdgcn_mfma_f32_16x16x32_bf16(a, b, c, 0, 0, 0);
}
__device__ __forceinline__ bf16x8 lds_a(const char* p) { return *(const bf16x8*)p; }
__device__ __forceinline__ bf16x8 lds_b(const char* p0, const char* p1) {
  s16x4 a = __builtin_amdgcn_ds_read_tr16_b64_v4i16((LDS_AS s16x4*)p0);
  s16x4 b = __builtin_amdgcn_ds_read_tr16_b64_v4i16((LDS_AS s16x4*)p1);
  s16x8 f = __builtin_shufflevector(a, b, 0, 1, 2, 3, 4, 5, 6, 7);
  return __builtin_bit_cast(bf16x8, f);
}
__device__ __forceinline__ float sumsq8(bf16x8 a, float c) {
  bf16x2 p0 = __builtin_shufflevector(a, a, 0, 1);
  bf16x2 p1 = __builtin_shufflevector(a, a, 2, 3);
  bf16x2 p2 = __builtin_shufflevector(a, a, 4, 5);
  bf16x2 p3 = __builtin_shufflevector(a, a, 6, 7);
  c = __builtin_amdgcn_fdot2_f32_bf16(p0, p0, c, false);
  c = __builtin_amdgcn_fdot2_f32_bf16(p1, p1, c, false);
  c = __builtin_amdgcn_fdot2_f32_bf16(p2, p2, c, false);
  c = __builtin_amdgcn_fdot2_f32_bf16(p3, p3, c, false);
  return c;
}
__device__ __forceinline__ float dot2u(unsigned a, unsigned b, float c) {
  return __builtin_amdgcn_fdot2_f32_bf16(__builtin_bit_cast(bf16x2, a), __builtin_bit_cast(bf16x2, b), c, false);
}
__device__ __forceinline__ float dot8(u32x4 a, u32x4 b) {
  const unsigned a0 = a[0], a1 = a[1], a2 = a[2], a3 = a[3];
  const unsigned b0 = b[0], b1 = b[1], b2 = b[2], b3 = b[3];
  float c = 0.f;
  c = dot2u(a0, b0, c);
  c = dot2u(a1, b1, c);
  c = dot2u(a2, b2, c);
  c = dot2u(a3, b3, c);
  return c;
}
__device__ __forceinline__ bf16x8 ones8() {
  s16x8 o = {0x3F80, 0x3F80, 0x3F80, 0x3F80, 0x3F80, 0x3F80, 0x3F80, 0x3F80};
  return __builtin_bit_cast(bf16x8, o);
}
__device__ __forceinline__ void glds16(const void* g, char* l) {
  __builtin_amdgcn_global_load_lds((const unsigned*)g, (unsigned*)l, 16, 0, 0);
}

#define ST_AUX 0
__device__ __forceinline__ void st16(void* base, unsigned off, u32x4 v) {
  __amdgpu_buffer_rsrc_t r = __builtin_amdgcn_make_buffer_rsrc(base, 0, 0x7fffffff, 0x00020000);
  __builtin_amdgcn_raw_buffer_store_b128(v, r, off, 0, ST_AUX);
}
__device__ __forceinline__ void swap16(unsigned& a, unsigned& b) {
  auto r = __builtin_amdgcn_permlane16_swap(a, b, false, false);
  a = r[0];
  b = r[1];
}

static constexpr int K1_STAGE = 30 * 1024;
#define K1R 4
static constexpr int K1_R = K1R;
static constexpr int K1_ABYTES = 18 * 1024;
static constexpr int K1_STAT = K1_R * K1_STAGE;
static constexpr int K1_COLP = K1_STAT + 144 * 8;
static constexpr int K1_HPART = K1_COLP + 4 * 96 * 4;
static constexpr int K1_ONES = K1_HPART + 4 * 16 * 8;
static constexpr int K1_LDS = K1_ONES + 16;
static constexpr int QKV_PITCH = 208;

__device__ __forceinline__ void k1_body(const u16* __restrict__ x, const u16* __restrict__ wqkv, u16* __restrict__ o,
                                        const u16* __restrict__ wo, const u16* __restrict__ w1,
                                        const u16* __restrict__ w2, const u16* __restrict__ outp, char* smem) {
  const int tid = threadIdx.x, lane = tid & 63;
  const int w = __builtin_amdgcn_readfirstlane(tid >> 6);
  const int rg = w >> 1, ch = w & 1;
  const int bid = blockIdx.x;
  const int panel = ((bid & 7) >> 1) * 4 + ((bid >> 3) & 3), hp = (bid & 1) * 8 + (bid >> 5);
  const int row0 = panel * 128 - 16;
  STAMPR(0, 14);
  STAMP(0, 0);
  const int g = lane >> 4, l15 = lane & 15, q4 = l15 >> 2, p4 = lane & 3;

  const char* src[4];
  int stride[4];
#pragma unroll
  for (int i = 0; i < 4; ++i) {
    const int p = w + 8 * i;
    if (p < 18) {
      const int rl = 8 * p + (lane >> 3);
      const int chunk = (lane & 7) ^ ((rl >> 1) & 7);
      int gr = row0 + rl;
      gr = gr < 0 ? 0 : gr;
      src[i] = (const char*)(x + (size_t)gr * D_ + chunk * 8);
      stride[i] = 128;
    } else {
      const int pb = (p < 30 ? p : 29) - 18;
      const int b = 4 * pb + g;
      const int c4 = b / 3, pr = b - 3 * c4;
      const int chunk = p4 ^ (2 * ((c4 >> 1) & 1));
      const int krow = 4 * c4 + q4;
      src[i] = (const char*)(wqkv + (size_t)krow * (3 * D_) + pr * D_ + hp * 32 + chunk * 8);
      stride[i] = 64 * 3 * D_ * 2;
    }
  }
  const bool has4 = (w < 6);
#define K0ROT ((bid >> 5) & 7)
  int kp = K0ROT;
#pragma unroll
  for (int i = 0; i < 4; ++i) src[i] += kp * stride[i];
  auto issue = [&](int slot) {
    char* base = smem + slot * K1_STAGE + w * 1024;
    const int mul = (kp == 7) ? -7 : 1;
    kp = (kp + 1) & 7;
#pragma unroll
    for (int i = 0; i < 3; ++i) {
      glds16(src[i], base + i * 8192);
      src[i] += mul * stride[i];
    }
    if (has4) {
      glds16(src[3], base + 3 * 8192);
      src[3] += mul * stride[3];
    }
  };

  const int mtA = 1 + 2 * rg + ch, mtB = 2 + 2 * rg - ch;
  const int aoff0 = l15 * 128 + ((g ^ (l15 >> 1)) * 16);
  const int boff = K1_ABYTES + g * 1536 + q4 * 64 + ((ch ^ (g & 1)) * 32) + p4 * 8;

  f32x4 acc[2][3], accR = {0, 0, 0, 0}, accG = {0, 0, 0, 0}, ex[2] = {f32x4{0, 0, 0, 0}, f32x4{0, 0, 0, 0}};
#pragma unroll
  for (int j = 0; j < 3; ++j) {
    acc[0][j] = f32x4{0, 0, 0, 0};
    acc[1][j] = f32x4{0, 0, 0, 0};
  }
  const bf16x8 ones = ones8();
  const bool odd = (rg & 1) != 0;
  const bool lowrg = rg < 2;
  const int jj1 = odd ? 2 : 1, jj2 = odd ? 1 : 2;
  const int boff1 = boff + jj1 * 256, boff2 = boff + jj2 * 256;

  const int kx = rg & 1;
  const int bko[2] = {kx * 6144, (kx ^ 1) * 6144};
  const int ako[2] = {aoff0 ^ (kx * 64), aoff0 ^ (kx * 64) ^ 64};
  const int hko[2] = {lowrg ? ako[0] : K1_ONES, lowrg ? ako[1] : K1_ONES};
  const bool qh = lowrg && ch == 1;
  const int qko = qh ? ako[0] : K1_ONES;
  if (tid < 4) *(unsigned*)(smem + K1_ONES + tid * 4) = 0x3F803F80u;
  struct Frag {
    bf16x8 a0, a1, ah, b[3];
  };
  bf16x8 fq;
  auto rd = [&](Frag& f, int slot, int i) {
    const char* st = smem + slot * K1_STAGE;
    f.a0 = lds_a(st + mtA * 2048 + ako[i]);
    f.b[0] = lds_b(st + boff + bko[i], st + boff + bko[i] + 768);
    f.b[1] = lds_b(st + boff1 + bko[i], st + boff1 + bko[i] + 768);
    f.b[2] = lds_b(st + boff2 + bko[i], st + boff2 + bko[i] + 768);
    f.a1 = lds_a(st + mtB * 2048 + ako[i]);
    f.ah = lds_a(smem + (lowrg ? slot * K1_STAGE : 0) + hko[i]);
    if (i == 0) fq = lds_a(smem + (qh ? slot * K1_STAGE : 0) + qko);
  };
  auto mm = [&](const Frag& f, int i) {
#pragma unroll
    for (int j = 0; j < 3; ++j) acc[0][j] = mfma16(f.b[j], f.a0, acc[0][j]);
    accR = mfma16(ones, f.a0, accR);
    accG = mfma16(f.a0, f.a0, accG);
#pragma unroll
    for (int j = 0; j < 3; ++j) acc[1][j] = mfma16(f.b[j], f.a1, acc[1][j]);
    ex[0] = mfma16(f.b[1], f.ah, ex[0]);
    if (i == 0) {
      const bf16x8 pp = lowrg ? f.ah : f.b[0];
      ex[1] = mfma16(pp, fq, ex[1]);
    }
  };

#pragma unroll
  for (int s = 0; s < K1_R - 1; ++s) issue(s);

  Frag fx, fy;
#pragma unroll
  for (int s = 0; s < 8; ++s) {
    const int rem = 7 - s;
    if (rem >= K1_R - 2) {
      if (has4) WAIT_V(4 * (K1_R - 2)); else WAIT_V(3 * (K1_R - 2));
    } else if (rem == 2) {
      if (has4) WAIT_V(8); else WAIT_V(6);
    } else if (rem == 1) {
      if (has4) WAIT_V(4); else WAIT_V(3);
    } else {
      WAIT_V(0);
    }
    RAW_BARRIER();
    STAMP(0, 1 + s);
    rd(fx, s % K1_R, 0);
    SCHED_PIN();
    if (s > 0) mm(fy, 1);
    SCHED_PIN();
    asm volatile("" ::"v"(fx.a0), "v"(fx.a1), "v"(fx.ah), "v"(fx.b[0]), "v"(fx.b[1]), "v"(fx.b[2]), "v"(fq));
    rd(fy, s % K1_R, 1);
    SCHED_PIN();
    mm(fx, 0);
    SCHED_PIN();
    if (s + K1_R - 1 < 8) issue((s + K1_R - 1) % K1_R);
    SCHED_PIN();
    asm volatile("" ::"v"(fy.a0), "v"(fy.a1), "v"(fy.ah), "v"(fy.b[0]), "v"(fy.b[1]), "v"(fy.b[2]));
  }
  mm(fy, 1);

  STAMP(0, 9);
  u32x4 wm0, wm1, wm2;
  {
    const int t16 = (bid * 512 + tid) * 16;
    const char* p0 = (t16 < 1792 * 1024) ? (const char*)w1 + t16 : (const char*)wo + (t16 - 1792 * 1024) ;
    const char* p1 = (t16 < 1792 * 1024) ? (const char*)w2 + t16 : (const char*)wo + (t16 - 1792 * 1024) + 256 * 1024;
    wm0 = *(const u32x4*)p0;
    wm1 = *(const u32x4*)p1;
    const int j = (bid >> 3) * 8 + (tid & 7);
    const char* p2 = (j < 176) ? (const char*)o + (size_t)(2 * 1024 * 1024) + (size_t)j * 65536 : (const char*)outp + (size_t)(j - 176) * 16384;
    wm2 = u32x4{0, 0, 0, 0};
    if (tid < 8) wm2 = *(const u32x4*)p2;
  }
  {
    float2* stat = (float2*)(smem + K1_STAT);
    float* colp = (float*)(smem + K1_COLP);
    float* hpart = (float*)(smem + K1_HPART);
    const int e = l15 & 3;
    const bool diag = (g == (l15 >> 2));
    const float gq = e == 0 ? accG[0] : (e == 1 ? accG[1] : (e == 2 ? accG[2] : accG[3]));
    if (diag) {
      const float mean = accR[0] * (1.f / D_);
      float var = gq * (1.f / D_) - mean * mean;
      var = var < 0.f ? 0.f : var;
      stat[mtA * 16 + l15] = make_float2(mean, rsqrtf(var + EPS));
    }
    if (lowrg) {
      const float hq = e == 0 ? ex[1][0] : (e == 1 ? ex[1][1] : (e == 2 ? ex[1][2] : ex[1][3]));
      if (diag) hpart[ch * 32 + rg * 16 + l15] = hq;
    } else if (l15 == 0) {
      *(f32x4*)(colp + (rg - 2) * 32 + ch * 16 + 4 * g) = ex[1];
      *(f32x4*)(colp + rg * 32 + ch * 16 + 4 * g) = ex[0];
    }
  }
  RAW_BARRIER();
  {
    const float2* stat = (const float2*)(smem + K1_STAT);
    const float* colp = (const float*)(smem + K1_COLP);
    f32x4 cs[3];
    int cj[3];
    cj[0] = (0 + ch) * 16 + 4 * g;
    cj[1] = (2 * jj1 + ch) * 16 + 4 * g;
    cj[2] = (2 * jj2 + ch) * 16 + 4 * g;
    cs[0] = *(const f32x4*)(colp + ch * 16 + 4 * g) + *(const f32x4*)(colp + 32 + ch * 16 + 4 * g);
    cs[1] = *(const f32x4*)(colp + (1 + jj1) * 32 + ch * 16 + 4 * g);
    cs[2] = *(const f32x4*)(colp + (1 + jj2) * 32 + ch * 16 + 4 * g);
#pragma unroll
    for (int i = 0; i < 2; ++i) {
      const int rl = (i == 0 ? mtA : mtB) * 16 + l15;
      const float2 sv = stat[rl];
#pragma unroll
      for (int j = 0; j < 3; ++j) {
        f32x4 v = (acc[i][j] - sv.x * cs[j]) * sv.y;
        u32x2 pk = {pack2(v[0], v[1]), pack2(v[2], v[3])};
        *(u32x2*)(smem + rl * QKV_PITCH + cj[j] * 2) = pk;
      }
    }
    if (lowrg) {
      const float* hpart = (const float*)(smem + K1_HPART);
      float2 sv;
      sv.x = (hpart[l15] + hpart[16 + l15]) * (1.f / D_);
      float var = (hpart[32 + l15] + hpart[48 + l15]) * (1.f / D_) - sv.x * sv.x;
      var = var < 0.f ? 0.f : var;
      sv.y = rsqrtf(var + EPS);
      f32x4 v = (ex[0] - sv.x * cs[1]) * sv.y;
      u32x2 pk = {pack2(v[0], v[1]), pack2(v[2], v[3])};
      *(u32x2*)(smem + l15 * QKV_PITCH + cj[1] * 2) = pk;
    }
  }
  RAW_BARRIER();
  STAMP(0, 10);
  {
    const int half = tid & 1, head = (tid >> 1) & 1, r = tid >> 2;
    const char* base = smem + (r + 16) * QKV_PITCH + (head * 16 + half * 8) * 2;
    const u32x4 qv = *(const u32x4*)base;
    const int grow = panel * 128 + r;
    float lg[8];
#pragma unroll
    for (int wi = 0; wi < 8; ++wi) {
      const u32x4 kv = *(const u32x4*)(base - 2 * wi * QKV_PITCH + 64);
      float d = dot8(qv, kv);
      d += __shfl_xor(d, 1);
      lg[wi] = (grow - 2 * wi >= 0) ? d * 0.25f : -1e9f;
    }
    float mx = lg[0];
#pragma unroll
    for (int wi = 1; wi < 8; ++wi) mx = fmaxf(mx, lg[wi]);
    float sum = 0.f;
#pragma unroll
    for (int wi = 0; wi < 8; ++wi) {
      lg[wi] = __expf(lg[wi] - mx);
      sum += lg[wi];
    }
    const float inv = __builtin_amdgcn_rcpf(sum);
    float ov[8];
#pragma unroll
    for (int d = 0; d < 8; ++d) ov[d] = 0.f;
#pragma unroll
    for (int wi = 0; wi < 8; ++wi) {
      const u32x4 vv = *(const u32x4*)(base - 2 * wi * QKV_PITCH + 128);
      const float p = lg[wi];
#pragma unroll
      for (int d = 0; d < 4; ++d) {
        ov[2 * d] = fmaf(p, bflo(vv[d]), ov[2 * d]);
        ov[2 * d + 1] = fmaf(p, bfhi(vv[d]), ov[2 * d + 1]);
      }
    }
    u32x4 out;
#pragma unroll
    for (int d = 0; d < 4; ++d) out[d] = pack2(ov[2 * d] * inv, ov[2 * d + 1] * inv);
    *(u32x4*)(o + (size_t)grow * D_ + hp * 32 + head * 16 + half * 8) = out;
  }
  asm volatile("" ::"v"(wm0), "v"(wm1), "v"(wm2));
  STAMP(0, 11);
  STAMPR(0, 15);
}

__global__ __launch_bounds__(512, 2) void k1_qkv_attn(const u16* __restrict__ x, const u16* __restrict__ wqkv,
                                                      u16* __restrict__ o, const u16* __restrict__ wo,
                                                      const u16* __restrict__ w1, const u16* __restrict__ w2,
                                                      const u16* __restrict__ outp) {
  extern __shared__ __attribute__((aligned(1024))) char smem[];
  k1_body(x, wqkv, o, wo, w1, w2, outp, smem);
}

static constexpr int G_STAGE = 16 * 1024;
static constexpr int G_R = 8;
static constexpr int G_LDS = G_R * G_STAGE;

template <int K, bool BIAS>
__device__ __forceinline__ void gemm64_body(const u16* __restrict__ A, const u16* __restrict__ W,
                                            const u16* __restrict__ res, const u16* __restrict__ bias,
                                            u16* __restrict__ out, float* __restrict__ stats, char* smem) {
  constexpr int NS = K / 64;
  constexpr int KID = BIAS ? 3 : 1;
  STAMPR(KID, 14);
  STAMP(KID, 0);
  const int tid = threadIdx.x, lane = tid & 63;
  const int w = __builtin_amdgcn_readfirstlane(tid >> 6);
  const int rg = w & 3, ch = w >> 2;
  const int bid = blockIdx.x;
  const int rt = (bid & 7) * 4 + (bid >> 6), ct = (bid >> 3) & 7;
  const int g = lane >> 4, l15 = lane & 15, q4 = l15 >> 2, p4 = lane & 3;

  const int row = rt * 64 + rg * 16 + l15;
  const int col8 = ct * 64 + ch * 32 + (g & 1) * 16 + (g >> 1) * 8;
  u32x4 rv = *(const u32x4*)(res + (size_t)row * D_ + col8);
  u32x4 bv = {0, 0, 0, 0};
  if (BIAS) bv = *(const u32x4*)(bias + col8);

  const char *srcA, *srcB;
  {
    const int rl = 8 * w + (lane >> 3);
    const int chunk = (lane & 7) ^ ((rl >> 1) & 7);
    srcA = (const char*)(A + (size_t)(rt * 64 + rl) * K + chunk * 8);
    const int b = 4 * w + g;
    const int c4 = b >> 1, pr = b & 1;
    const int chunkb = p4 ^ (2 * ((c4 >> 1) & 1));
    const int krow = 4 * c4 + q4;
    srcB = (const char*)(W + (size_t)krow * D_ + ct * 64 + pr * 32 + chunkb * 8);
  }
  auto issue = [&](int slot) {
    char* base = smem + slot * G_STAGE + w * 1024;
    glds16(srcA, base);
    glds16(srcB, base + 8192);
    srcA += 128;
    srcB += 64 * D_ * 2;
  };
  const int aoff0 = rg * 2048 + l15 * 128 + ((g ^ (l15 >> 1)) * 16);
  const int boff = 8192 + g * 1024 + ch * 256 + q4 * 64 + p4 * 8;
  const int sw = (g & 1) * 32;

  f32x4 acc[2] = {f32x4{0, 0, 0, 0}, f32x4{0, 0, 0, 0}};

#pragma unroll
  for (int s = 0; s < G_R - 1; ++s) issue(s);

  auto compute = [&](int slot) {
    const char* st = smem + slot * G_STAGE;
#pragma unroll
    for (int ks = 0; ks < 2; ++ks) {
      bf16x8 a = lds_a(st + (aoff0 ^ (ks * 64)));
#pragma unroll
      for (int j = 0; j < 2; ++j) {
        const int o2 = boff + ((j * 32) ^ sw);
        bf16x8 b = lds_b(st + o2 + (ks * 8 + 0) * 512, st + o2 + (ks * 8 + 1) * 512);
        acc[j] = mfma16(b, a, acc[j]);
      }
    }
  };

  STAMP(KID, 1);
  constexpr int NMAIN = NS - (G_R - 1);
  int slot = 0;
  for (int s = 0; s < NMAIN; ++s) {
    WAIT_V(2 * (G_R - 2));
    RAW_BARRIER();
    if (s == 0) STAMP(KID, 2);
    issue((slot + G_R - 1) & (G_R - 1));
    compute(slot);
    slot = (slot + 1) & (G_R - 1);
  }
#pragma unroll
  for (int t = 0; t < G_R - 1; ++t) {
    switch (G_R - 2 - t) {
      case 0: WAIT_V(0); break;
      case 1: WAIT_V(2); break;
      case 2: WAIT_V(4); break;
      case 3: WAIT_V(6); break;
      case 4: WAIT_V(8); break;
      case 5: WAIT_V(10); break;
      default: WAIT_V(12); break;
    }
    RAW_BARRIER();
    compute(slot);
    slot = (slot + 1) & (G_R - 1);
  }

  STAMP(KID, 3);
  {
    unsigned r0 = rv[0], r1 = rv[1], r2 = rv[2], r3 = rv[3];
    swap16(r0, r2);
    swap16(r1, r3);
    f32x4 v0 = acc[0], v1 = acc[1];
    v0[0] += bflo(r0);
    v0[1] += bfhi(r0);
    v0[2] += bflo(r1);
    v0[3] += bfhi(r1);
    v1[0] += bflo(r2);
    v1[1] += bfhi(r2);
    v1[2] += bflo(r3);
    v1[3] += bfhi(r3);
    if (BIAS) {
      unsigned b0 = bv[0], b1 = bv[1], b2 = bv[2], b3 = bv[3];
      swap16(b0, b2);
      swap16(b1, b3);
      v0[0] += bflo(b0);
      v0[1] += bfhi(b0);
      v0[2] += bflo(b1);
      v0[3] += bfhi(b1);
      v1[0] += bflo(b2);
      v1[1] += bfhi(b2);
      v1[2] += bflo(b3);
      v1[3] += bfhi(b3);
    }
    unsigned p0 = pack2(v0[0], v0[1]), p1 = pack2(v0[2], v0[3]);
    unsigned p2 = pack2(v1[0], v1[1]), p3 = pack2(v1[2], v1[3]);
    if (!BIAS) {
      float sm = dot2u(p0, 0x3F803F80u, 0.f), sq = dot2u(p0, p0, 0.f);
      sm = dot2u(p1, 0x3F803F80u, sm);
      sq = dot2u(p1, p1, sq);
      sm = dot2u(p2, 0x3F803F80u, sm);
      sq = dot2u(p2, p2, sq);
      sm = dot2u(p3, 0x3F803F80u, sm);
      sq = dot2u(p3, p3, sq);
      sm += __shfl_xor(sm, 16);
      sq += __shfl_xor(sq, 16);
      sm += __shfl_xor(sm, 32);
      sq += __shfl_xor(sq, 32);
      if (g == 0) *(float2*)(stats + (size_t)row * 32 + (ct * 2 + ch) * 2) = make_float2(sm, sq);
    }
    swap16(p0, p2);
    swap16(p1, p3);
    *(u32x4*)(out + (size_t)row * D_ + col8) = u32x4{p0, p1, p2, p3};
  }
  STAMP(KID, 4);
  STAMPR(KID, 15);
}

template <int K, bool BIAS>
__global__ __launch_bounds__(512, 2) void k_gemm64(const u16* __restrict__ A, const u16* __restrict__ W,
                                                   const u16* __restrict__ res, const u16* __restrict__ bias,
                                                   u16* __restrict__ out, float* __restrict__ stats) {
  extern __shared__ __attribute__((aligned(1024))) char smem[];
  gemm64_body<K, BIAS>(A, W, res, bias, out, stats, smem);
}

static constexpr int M1_RING = 65536;
static constexpr int M1_XCH = M1_RING + 5 * 16384;
static constexpr int K3_LDS = M1_XCH + 2048;

__device__ __forceinline__ float gelu_tanh(float v) {
  const float p = fmaf(v * v, -2.f * 1.4426950408889634f * 0.7978845608028654f * 0.044715f,
                       -2.f * 1.4426950408889634f * 0.7978845608028654f);
  const float e = __builtin_amdgcn_exp2f(v * p);
  return v * __builtin_amdgcn_rcpf(1.f + e);
}
__device__ __forceinline__ f32x4 ld16_async(const void* p) {
  f32x4 r;
  asm volatile("global_load_dwordx4 %0, %1, off" : "=v"(r) : "v"(p) : "memory");
  return r;
}
__device__ __forceinline__ u32x2 ld8_async(const void* p) {
  u32x2 r;
  asm volatile("global_load_dwordx2 %0, %1, off" : "=v"(r) : "v"(p) : "memory");
  return r;
}

__device__ __forceinline__ void mlp1_body(const u16* __restrict__ h, const u16* __restrict__ w1,
                                          const u16* __restrict__ b1, const float* __restrict__ stats,
                                          u16* __restrict__ h1, char* smem) {
  const int tid = threadIdx.x, lane = tid & 63;
  const int w = __builtin_amdgcn_readfirstlane(tid >> 6);
  const int cg = w & 3, rgp = w >> 2;
  const int bid = blockIdx.x;
  const int xcd = bid & 7, idx = bid >> 3;
  const int pan = (xcd >> 1) * 4 + (idx & 3), ct = (xcd & 1) * 7 + (idx >> 2);
  const int R0 = pan * 128, C0 = ct * 128;
  const int g = lane >> 4, l15 = lane & 15, q4 = l15 >> 2, p4 = lane & 3;
  STAMPR(2, 14);
  STAMP(2, 0);

  f32x4 sp[2];
#pragma unroll
  for (int i = 0; i < 2; ++i) sp[i] = ld16_async(stats + (size_t)(R0 + 16 * w + 8 * i + (lane >> 3)) * 32 + (lane & 7) * 4);
  u32x2 bv[2];
#pragma unroll
  for (int j = 0; j < 2; ++j) bv[j] = ld8_async(b1 + C0 + cg * 32 + j * 16 + 4 * g);

  const char* srcK[2];
  const char* srcA[2];
  const char* srcB[2];
#pragma unroll
  for (int r = 0; r < 2; ++r) {
    {
      const int rl = 8 * w + 4 * r + g;
      const int chunk = l15 ^ (rl & 15);
      srcK[r] = (const char*)(h + (size_t)(R0 + rl) * D_ + chunk * 8);
    }
    {
      const int rl = 2 * w + r;
      const int chunk = (lane & 48) | ((lane & 15) ^ rl);
      srcA[r] = (const char*)(h + (size_t)(R0 + 64 + rl) * D_ + chunk * 8);
    }
    const int b = 4 * (w + 8 * r) + g;
    const int c4 = b >> 2, pr = b & 3;
    const int chunkb = p4 ^ (2 * ((c4 >> 1) & 1));
    srcB[r] = (const char*)(w1 + (size_t)(4 * c4 + q4) * F_ + C0 + pr * 32 + chunkb * 8);
  }
  auto issueK = [&](char* dst) {
    glds16(srcK[0], dst + w * 2048);
    glds16(srcK[1], dst + w * 2048 + 1024);
    srcK[0] += 256;
    srcK[1] += 256;
  };
  auto issueA = [&](char* dst) {
    glds16(srcA[0], dst + w * 2048);
    glds16(srcA[1], dst + w * 2048 + 1024);
    srcA[0] += 16 * D_ * 2;
    srcA[1] += 16 * D_ * 2;
  };
  auto issueB = [&](char* dst) {
    glds16(srcB[0], dst + w * 1024);
    glds16(srcB[1], dst + w * 1024 + 8192);
    srcB[0] += 64 * F_ * 2;
    srcB[1] += 64 * F_ * 2;
  };
  char* ring = smem + M1_RING;
  issueK(smem);
  issueB(ring);
  issueB(ring + 16384);
  issueK(smem + 16384);

  int aoff[4], koff[4];
#pragma unroll
  for (int a = 0; a < 4; ++a) {
    aoff[a] = l15 * 1024 + ((g ^ l15 ^ (4 * a)) * 16);
    koff[a] = rgp * 4096 + l15 * 256 + ((g ^ l15 ^ (4 * a)) * 16);
  }
  const int boff = g * 2048 + cg * 256 + q4 * 64 + p4 * 8;
  const int sw = (g & 1) * 32;

  asm volatile("s_waitcnt vmcnt(8)" : "+v"(sp[0]), "+v"(sp[1]), "+v"(bv[0]), "+v"(bv[1]) : : "memory");
  float2* rstat = (float2*)(smem + M1_XCH + 1024);
#pragma unroll
  for (int i = 0; i < 2; ++i) {
    float sm = sp[i][0] + sp[i][2];
    float sq = sp[i][1] + sp[i][3];
#pragma unroll
    for (int m = 1; m < 8; m <<= 1) {
      sm += __shfl_xor(sm, m);
      sq += __shfl_xor(sq, m);
    }
    const float mean = sm * (1.f / D_);
    float var = sq * (1.f / D_) - mean * mean;
    var = var < 0.f ? 0.f : var;
    const float r1 = rsqrtf(var + EPS);
    const float v1 = var * r1 * r1;
    const float r2 = rsqrtf(v1 + EPS);
    const float rs = r1 * r2;
    if ((lane & 7) == 0) rstat[16 * w + 8 * i + (lane >> 3)] = make_float2(rs, -mean * rs);
  }

  bf16x8 bf[16][2];
  f32x4 acc[4][2], accC[2];
#pragma unroll
  for (int t = 0; t < 4; ++t) {
    acc[t][0] = f32x4{0, 0, 0, 0};
    acc[t][1] = f32x4{0, 0, 0, 0};
  }
  accC[0] = f32x4{0, 0, 0, 0};
  accC[1] = f32x4{0, 0, 0, 0};
  const bf16x8 ones = ones8();
  STAMP(2, 1);

#pragma unroll
  for (int s = 0; s < 8; ++s) {
    if (s & 1) WAIT_V(6); else WAIT_V(4);
    RAW_BARRIER();
    if (s == 0) STAMP(2, 2);
    const char* st = ring + (s % 5) * 16384;
#pragma unroll
    for (int ks = 0; ks < 2; ++ks) {
      const int kk = 2 * s + ks;
#pragma unroll
      for (int j = 0; j < 2; ++j) {
        const int o2 = boff + ((j * 32) ^ sw);
        bf[kk][j] = lds_b(st + o2 + (ks * 8 + 0) * 1024, st + o2 + (ks * 8 + 1) * 1024);
      }
      const char* ap = smem + (kk >> 2) * 16384 + koff[kk & 3];
      const bf16x8 a0 = lds_a(ap), a1 = lds_a(ap + 8192);
#pragma unroll
      for (int j = 0; j < 2; ++j) {
        acc[0][j] = mfma16(bf[kk][j], a0, acc[0][j]);
        acc[1][j] = mfma16(bf[kk][j], a1, acc[1][j]);
      }
    }
#pragma unroll
    for (int j = 0; j < 2; ++j) {
      const bf16x8 bs = rgp ? bf[2 * s + 1][j] : bf[2 * s][j];
      accC[j] = mfma16(bs, ones, accC[j]);
    }
    switch (s) {
      case 0: issueB(ring + 2 * 16384); issueB(ring + 3 * 16384); break;
      case 1: issueK(smem + 2 * 16384); break;
      case 2: issueB(ring + 4 * 16384); issueB(ring); break;
      case 3: issueK(smem + 3 * 16384); break;
      case 4: issueB(ring + 16384); issueB(ring + 2 * 16384); break;
      case 5: issueA(ring + 3 * 16384); break;
      case 6: issueA(ring + 4 * 16384); issueA(ring); break;
      default: issueA(ring + 16384); break;
    }
  }
  STAMP(2, 3);
  float* xch = (float*)(smem + M1_XCH);
  if (l15 == 0) {
    *(f32x4*)(xch + w * 32 + 4 * g) = accC[0];
    *(f32x4*)(xch + w * 32 + 16 + 4 * g) = accC[1];
  }
  WAIT_V(4);
  RAW_BARRIER();
  f32x4 tb[2];
  f32x4 cs[2];
#pragma unroll
  for (int j = 0; j < 2; ++j) {
    cs[j] = accC[j] + *(const f32x4*)(xch + (w ^ 4) * 32 + j * 16 + 4 * g);
    tb[j] = f32x4{bflo(bv[j][0]), bfhi(bv[j][0]), bflo(bv[j][1]), bfhi(bv[j][1])};
  }
  auto epi = [&](int t) {
    const float2 rm = rstat[(rgp + 2 * t) * 16 + l15];
    unsigned pk[2][2];
#pragma unroll
    for (int j = 0; j < 2; ++j) {
      float o4[4];
#pragma unroll
      for (int i = 0; i < 4; ++i) {
        const float tt = fmaf(rm.y, cs[j][i], tb[j][i]);
        o4[i] = gelu_tanh(fmaf(acc[t][j][i], rm.x, tt));
      }
      pk[j][0] = pack2(o4[0], o4[1]);
      pk[j][1] = pack2(o4[2], o4[3]);
    }
    swap16(pk[0][0], pk[1][0]);
    swap16(pk[0][1], pk[1][1]);
    const int row = R0 + (rgp + 2 * t) * 16 + l15;
    st16(h1, (unsigned)(row * F_ + C0 + cg * 32 + (g & 1) * 16 + (g >> 1) * 8) * 2u,
         u32x4{pk[0][0], pk[0][1], pk[1][0], pk[1][1]});
  };
  auto mm = [&](int t, const char* base) {
#pragma unroll
    for (int kk = 0; kk < 16; ++kk) {
      const bf16x8 a = lds_a(base + aoff[kk & 3] + (kk >> 2) * 256);
      acc[t][0] = mfma16(bf[kk][0], a, acc[t][0]);
      acc[t][1] = mfma16(bf[kk][1], a, acc[t][1]);
    }
  };
  if (rgp == 0) {
    epi(0);
    mm(2, ring + 3 * 16384);
    epi(1);
    WAIT_V(2);
    RAW_BARRIER();
    STAMP(2, 4);
    mm(3, ring + 0 * 16384);
    epi(2);
    epi(3);
  } else {
    mm(2, ring + 4 * 16384);
    epi(0);
    WAIT_V(1);
    RAW_BARRIER();
    epi(1);
    mm(3, ring + 1 * 16384);
    epi(2);
    epi(3);
  }
  STAMP(2, 5);
  STAMPR(2, 15);
}

__global__ __launch_bounds__(512, 2) void k3_mlp1(const u16* __restrict__ h, const u16* __restrict__ w1,
                                                  const u16* __restrict__ b1, const float* __restrict__ stats,
                                                  u16* __restrict__ h1) {
  extern __shared__ __attribute__((aligned(1024))) char smem[];
  mlp1_body(h, w1, b1, stats, h1, smem);
}

extern "C" void kernel_launch(void* const* d_in, const int* in_sizes, int n_in, void* d_out, int out_size, void* d_ws,
                              size_t ws_size, hipStream_t stream) {
  const u16* x = (const u16*)d_in[0];
  const u16* wqkv = (const u16*)d_in[1];
  const u16* wo = (const u16*)d_in[2];
  const u16* w1 = (const u16*)d_in[4];
  const u16* b1 = (const u16*)d_in[5];
  const u16* w2 = (const u16*)d_in[6];
  const u16* b2 = (const u16*)d_in[7];
  u16* out = (u16*)d_out;
  char* ws = (char*)d_ws;
  u16* o = (u16*)ws;
  u16* h = (u16*)(ws + (size_t)2 * 1024 * 1024);
  u16* h1 = (u16*)(ws + (size_t)4 * 1024 * 1024);
  float* stats = (float*)(ws + (size_t)12 * 1024 * 1024);

  static_assert(K1_LDS <= 160 * 1024 && G_LDS <= 160 * 1024 && K3_LDS <= 160 * 1024, "lds");
  (void)hipFuncSetAttribute((const void*)k1_qkv_attn, hipFuncAttributeMaxDynamicSharedMemorySize, K1_LDS);
  (void)hipFuncSetAttribute((const void*)k_gemm64<512, false>, hipFuncAttributeMaxDynamicSharedMemorySize, G_LDS);
  (void)hipFuncSetAttribute((const void*)k3_mlp1, hipFuncAttributeMaxDynamicSharedMemorySize, K3_LDS);
  (void)hipFuncSetAttribute((const void*)k_gemm64<1792, true>, hipFuncAttributeMaxDynamicSharedMemorySize, G_LDS);

  k1_qkv_attn<<<256, 512, K1_LDS, stream>>>(x, wqkv, o, wo, w1, w2, out);
  k_gemm64<512, false><<<256, 512, G_LDS, stream>>>(o, wo, x, b2, h, stats);
  k3_mlp1<<<224, 512, K3_LDS, stream>>>(h, w1, b1, stats, h1);
  k_gemm64<1792, true><<<256, 512, G_LDS, stream>>>(h1, w2, h, b2, out, stats);
}
```

```cpp
#include <hip/hip_runtime.h>
#include <stdint.h>

typedef __attribute__((ext_vector_type(4))) short s16x4;
typedef __attribute__((ext_vector_type(8))) short s16x8;
typedef __attribute__((ext_vector_type(2))) __bf16 bf16x2;
typedef __attribute__((ext_vector_type(8))) __bf16 bf16x8;
typedef __attribute__((ext_vector_type(4))) float f32x4;
typedef __attribute__((ext_vector_type(4))) unsigned u32x4;
typedef __attribute__((ext_vector_type(2))) unsigned u32x2;
typedef unsigned short u16;
#define LDS_AS __attribute__((address_space(3)))

#define WAIT_V(n) asm volatile("s_waitcnt vmcnt(%0)" ::"n"(n) : "memory")
#define WAIT_L0() asm volatile("s_waitcnt lgkmcnt(0)" ::: "memory")
#define RAW_BARRIER()                  \
  do {                                 \
    WAIT_L0();                         \
    __builtin_amdgcn_s_barrier();      \
    asm volatile("" ::: "memory");     \
  } while (0)

#define SCHED_PIN() __builtin_amdgcn_sched_barrier(0)
#define STAMP(kid, i)
#define STAMPR(kid, i)
static constexpr int S_ = 2048;
static constexpr int D_ = 512;
static constexpr int F_ = 1792;
static constexpr float EPS = 1e-5f;

__device__ __forceinline__ unsigned pack2(float a, float b) {
  bf16x2 v = {(__bf16)a, (__bf16)b};
  return __builtin_bit_cast(unsigned, v);
}
__device__ __forceinline__ float bflo(unsigned u) { return __uint_as_float(u << 16); }
__device__ __forceinline__ float bfhi(unsigned u) { return __uint_as_float(u & 0xffff0000u); }

__device__ __forceinline__ f32x4 mfma16(bf16x8 a, bf16x8 b, f32x4 c) {
  return __builtin_amdgcn_mfma_f32_16x16x32_bf16(a, b, c, 0, 0, 0);
}
__device__ __forceinline__ bf16x8 lds_a(const char* p) { return *(const bf16x8*)p; }
__device__ __forceinline__ bf16x8 lds_b(const char* p0, const char* p1) {
  s16x4 a = __builtin_amdgcn_ds_read_tr16_b64_v4i16((LDS_AS s16x4*)p0);
  s16x4 b = __builtin_amdgcn_ds_read_tr16_b64_v4i16((LDS_AS s16x4*)p1);
  s16x8 f = __builtin_shufflevector(a, b, 0, 1, 2, 3, 4, 5, 6, 7);
  return __builtin_bit_cast(bf16x8, f);
}
__device__ __forceinline__ float sumsq8(bf16x8 a, float c) {
  bf16x2 p0 = __builtin_shufflevector(a, a, 0, 1);
  bf16x2 p1 = __builtin_shufflevector(a, a, 2, 3);
  bf16x2 p2 = __builtin_shufflevector(a, a, 4, 5);
  bf16x2 p3 = __builtin_shufflevector(a, a, 6, 7);
  c = __builtin_amdgcn_fdot2_f32_bf16(p0, p0, c, false);
  c = __builtin_amdgcn_fdot2_f32_bf16(p1, p1, c, false);
  c = __builtin_amdgcn_fdot2_f32_bf16(p2, p2, c, false);
  c = __builtin_amdgcn_fdot2_f32_bf16(p3, p3, c, false);
  return c;
}
__device__ __forceinline__ float dot2u(unsigned a, unsigned b, float c) {
  return __builtin_amdgcn_fdot2_f32_bf16(__builtin_bit_cast(bf16x2, a), __builtin_bit_cast(bf16x2, b), c, false);
}
__device__ __forceinline__ float dot8(u32x4 a, u32x4 b) {
  const unsigned a0 = a[0], a1 = a[1], a2 = a[2], a3 = a[3];
  const unsigned b0 = b[0], b1 = b[1], b2 = b[2], b3 = b[3];
  float c = 0.f;
  c = dot2u(a0, b0, c);
  c = dot2u(a1, b1, c);
  c = dot2u(a2, b2, c);
  c = dot2u(a3, b3, c);
  return c;
}
__device__ __forceinline__ bf16x8 ones8() {
  s16x8 o = {0x3F80, 0x3F80, 0x3F80, 0x3F80, 0x3F80, 0x3F80, 0x3F80, 0x3F80};
  return __builtin_bit_cast(bf16x8, o);
}
__device__ __forceinline__ void glds16(const void* g, char* l) {
  __builtin_amdgcn_global_load_lds((const unsigned*)g, (unsigned*)l, 16, 0, 0);
}

#define ST_AUX 0
__device__ __forceinline__ void st16(void* base, unsigned off, u32x4 v) {
  __amdgpu_buffer_rsrc_t r = __builtin_amdgcn_make_buffer_rsrc(base, 0, 0x7fffffff, 0x00020000);
  __builtin_amdgcn_raw_buffer_store_b128(v, r, off, 0, ST_AUX);
}
__device__ __forceinline__ void swap16(unsigned& a, unsigned& b) {
  auto r = __builtin_amdgcn_permlane16_swap(a, b, false, false);
  a = r[0];
  b = r[1];
}

static constexpr int K1_STAGE = 30 * 1024;
#define K1R 4
static constexpr int K1_R = K1R;
static constexpr int K1_ABYTES = 18 * 1024;
static constexpr int K1_STAT = K1_R * K1_STAGE;
static constexpr int K1_COLP = K1_STAT + 144 * 8;
static constexpr int K1_HPART = K1_COLP + 4 * 96 * 4;
static constexpr int K1_ONES = K1_HPART + 4 * 16 * 8;
static constexpr int K1_LDS = K1_ONES + 16;
static constexpr int QKV_PITCH = 208;

__device__ __forceinline__ void k1_body(const u16* __restrict__ x, const u16* __restrict__ wqkv, u16* __restrict__ o,
                                        const u16* __restrict__ wo, const u16* __restrict__ w1,
                                        const u16* __restrict__ w2, const u16* __restrict__ outp, char* smem) {
  const int tid = threadIdx.x, lane = tid & 63;
  const int w = __builtin_amdgcn_readfirstlane(tid >> 6);
  const int rg = w >> 1, ch = w & 1;
  const int bid = blockIdx.x;
  const int panel = ((bid & 7) >> 1) * 4 + ((bid >> 3) & 3), hp = (bid & 1) * 8 + (bid >> 5);
  const int row0 = panel * 128 - 16;
  STAMPR(0, 14);
  STAMP(0, 0);
  const int g = lane >> 4, l15 = lane & 15, q4 = l15 >> 2, p4 = lane & 3;

  const char* src[4];
  int stride[4];
#pragma unroll
  for (int i = 0; i < 4; ++i) {
    const int p = w + 8 * i;
    if (p < 18) {
      const int rl = 8 * p + (lane >> 3);
      const int chunk = (lane & 7) ^ ((rl >> 1) & 7);
      int gr = row0 + rl;
      gr = gr < 0 ? 0 : gr;
      src[i] = (const char*)(x + (size_t)gr * D_ + chunk * 8);
      stride[i] = 128;
    } else {
      const int pb = (p < 30 ? p : 29) - 18;
      const int b = 4 * pb + g;
      const int c4 = b / 3, pr = b - 3 * c4;
      const int chunk = p4 ^ (2 * ((c4 >> 1) & 1));
      const int krow = 4 * c4 + q4;
      src[i] = (const char*)(wqkv + (size_t)krow * (3 * D_) + pr * D_ + hp * 32 + chunk * 8);
      stride[i] = 64 * 3 * D_ * 2;
    }
  }
  const bool has4 = (w < 6);
#define K0ROT ((bid >> 5) & 7)
  int kp = K0ROT;
#pragma unroll
  for (int i = 0; i < 4; ++i) src[i] += kp * stride[i];
  auto issue = [&](int slot) {
    char* base = smem + slot * K1_STAGE + w * 1024;
    const int mul = (kp == 7) ? -7 : 1;
    kp = (kp + 1) & 7;
#pragma unroll
    for (int i = 0; i < 3; ++i) {
      glds16(src[i], base + i * 8192);
      src[i] += mul * stride[i];
    }
    if (has4) {
      glds16(src[3], base + 3 * 8192);
      src[3] += mul * stride[3];
    }
  };

  const int mtA = 1 + 2 * rg + ch, mtB = 2 + 2 * rg - ch;
  const int aoff0 = l15 * 128 + ((g ^ (l15 >> 1)) * 16);
  const int boff = K1_ABYTES + g * 1536 + q4 * 64 + ((ch ^ (g & 1)) * 32) + p4 * 8;

  f32x4 acc[2][3], accR = {0, 0, 0, 0}, accG = {0, 0, 0, 0}, ex[2] = {f32x4{0, 0, 0, 0}, f32x4{0, 0, 0, 0}};
#pragma unroll
  for (int j = 0; j < 3; ++j) {
    acc[0][j] = f32x4{0, 0, 0, 0};
    acc[1][j] = f32x4{0, 0, 0, 0};
  }
  const bf16x8 ones = ones8();
  const bool odd = (rg & 1) != 0;
  const bool lowrg = rg < 2;
  const int jj1 = odd ? 2 : 1, jj2 = odd ? 1 : 2;
  const int boff1 = boff + jj1 * 256, boff2 = boff + jj2 * 256;

  const int kx = rg & 1;
  const int bko[2] = {kx * 6144, (kx ^ 1) * 6144};
  const int ako[2] = {aoff0 ^ (kx * 64), aoff0 ^ (kx * 64) ^ 64};
  const int hko[2] = {lowrg ? ako[0] : K1_ONES, lowrg ? ako[1] : K1_ONES};
  const bool qh = lowrg && ch == 1;
  const int qko = qh ? ako[0] : K1_ONES;
  if (tid < 4) *(unsigned*)(smem + K1_ONES + tid * 4) = 0x3F803F80u;
  struct Frag {
    bf16x8 a0, a1, ah, b[3];
  };
  bf16x8 fq;
  auto rd = [&](Frag& f, int slot, int i) {
    const char* st = smem + slot * K1_STAGE;
    f.a0 = lds_a(st + mtA * 2048 + ako[i]);
    f.b[0] = lds_b(st + boff + bko[i], st + boff + bko[i] + 768);
    f.b[1] = lds_b(st + boff1 + bko[i], st + boff1 + bko[i] + 768);
    f.b[2] = lds_b(st + boff2 + bko[i], st + boff2 + bko[i] + 768);
    f.a1 = lds_a(st + mtB * 2048 + ako[i]);
    f.ah = lds_a(smem + (lowrg ? slot * K1_STAGE : 0) + hko[i]);
    if (i == 0) fq = lds_a(smem + (qh ? slot * K1_STAGE : 0) + qko);
  };
  auto mm = [&](const Frag& f, int i) {
#pragma unroll
    for (int j = 0; j < 3; ++j) acc[0][j] = mfma16(f.b[j], f.a0, acc[0][j]);
    accR = mfma16(ones, f.a0, accR);
    accG = mfma16(f.a0, f.a0, accG);
#pragma unroll
    for (int j = 0; j < 3; ++j) acc[1][j] = mfma16(f.b[j], f.a1, acc[1][j]);
    ex[0] = mfma16(f.b[1], f.ah, ex[0]);
    if (i == 0) {
      const bf16x8 pp = lowrg ? f.ah : f.b[0];
      ex[1] = mfma16(pp, fq, ex[1]);
    }
  };

#pragma unroll
  for (int s = 0; s < K1_R - 1; ++s) issue(s);

  Frag fx, fy;
#pragma unroll
  for (int s = 0; s < 8; ++s) {
    const int rem = 7 - s;
    if (rem >= K1_R - 2) {
      if (has4) WAIT_V(4 * (K1_R - 2)); else WAIT_V(3 * (K1_R - 2));
    } else if (rem == 2) {
      if (has4) WAIT_V(8); else WAIT_V(6);
    } else if (rem == 1) {
      if (has4) WAIT_V(4); else WAIT_V(3);
    } else {
      WAIT_V(0);
    }
    RAW_BARRIER();
    STAMP(0, 1 + s);
    rd(fx, s % K1_R, 0);
    SCHED_PIN();
    if (s > 0) mm(fy, 1);
    SCHED_PIN();
    asm volatile("" ::"v"(fx.a0), "v"(fx.a1), "v"(fx.ah), "v"(fx.b[0]), "v"(fx.b[1]), "v"(fx.b[2]), "v"(fq));
    rd(fy, s % K1_R, 1);
    SCHED_PIN();
    mm(fx, 0);
    SCHED_PIN();
    if (s + K1_R - 1 < 8) issue((s + K1_R - 1) % K1_R);
    SCHED_PIN();
    asm volatile("" ::"v"(fy.a0), "v"(fy.a1), "v"(fy.ah), "v"(fy.b[0]), "v"(fy.b[1]), "v"(fy.b[2]));
  }
  mm(fy, 1);

  STAMP(0, 9);
  u32x4 wm0, wm1, wm2;
  {
    const int t16 = (bid * 512 + tid) * 16;
    const char* p0 = (t16 < 1792 * 1024) ? (const char*)w1 + t16 : (const char*)wo + (t16 - 1792 * 1024) ;
    const char* p1 = (t16 < 1792 * 1024) ? (const char*)w2 + t16 : (const char*)wo + (t16 - 1792 * 1024) + 256 * 1024;
    wm0 = *(const u32x4*)p0;
    wm1 = *(const u32x4*)p1;
    const int j = (bid >> 3) * 8 + (tid & 7);
    const char* p2 = (j < 176) ? (const char*)o + (size_t)(2 * 1024 * 1024) + (size_t)j * 65536 : (const char*)outp + (size_t)(j - 176) * 16384;
    wm2 = u32x4{0, 0, 0, 0};
    if (tid < 8) wm2 = *(const u32x4*)p2;
  }
  {
    float2* stat = (float2*)(smem + K1_STAT);
    float* colp = (float*)(smem + K1_COLP);
    float* hpart = (float*)(smem + K1_HPART);
    const int e = l15 & 3;
    const bool diag = (g == (l15 >> 2));
    const float gq = e == 0 ? accG[0] : (e == 1 ? accG[1] : (e == 2 ? accG[2] : accG[3]));
    if (diag) {
      const float mean = accR[0] * (1.f / D_);
      float var = gq * (1.f / D_) - mean * mean;
      var = var < 0.f ? 0.f : var;
      stat[mtA * 16 + l15] = make_float2(mean, rsqrtf(var + EPS));
    }
    if (lowrg) {
      const float hq = e == 0 ? ex[1][0] : (e == 1 ? ex[1][1] : (e == 2 ? ex[1][2] : ex[1][3]));
      if (diag) hpart[ch * 32 + rg * 16 + l15] = hq;
    } else if (l15 == 0) {
      *(f32x4*)(colp + (rg - 2) * 32 + ch * 16 + 4 * g) = ex[1];
      *(f32x4*)(colp + rg * 32 + ch * 16 + 4 * g) = ex[0];
    }
  }
  RAW_BARRIER();
  u32x2 qf[2], kf[2];
  char* xk = smem;
  char* xv = smem + 9 * 1024;
  {
    const float2* stat = (const float2*)(smem + K1_STAT);
    const float* colp = (const float*)(smem + K1_COLP);
    const f32x4 cs0 = *(const f32x4*)(colp + ch * 16 + 4 * g) + *(const f32x4*)(colp + 32 + ch * 16 + 4 * g);
    const f32x4 cs1 = *(const f32x4*)(colp + (1 + jj1) * 32 + ch * 16 + 4 * g);
    const f32x4 cs2 = *(const f32x4*)(colp + (1 + jj2) * 32 + ch * 16 + 4 * g);
    const int kofs = ch * 512 + lane * 8, vofs = 9 * 1024 + ch * 512 + l15 * 32 + g * 8;
    const int ofs1 = odd ? vofs : kofs, ofs2 = odd ? kofs : vofs;
#pragma unroll
    for (int i = 0; i < 2; ++i) {
      const int mt = (i == 0 ? mtA : mtB);
      const float2 sv = stat[mt * 16 + l15];
      const f32x4 q = (acc[i][0] - sv.x * cs0) * sv.y;
      const f32x4 t1 = (acc[i][1] - sv.x * cs1) * sv.y;
      const f32x4 t2 = (acc[i][2] - sv.x * cs2) * sv.y;
      qf[i] = u32x2{pack2(q[0], q[1]), pack2(q[2], q[3])};
      const u32x2 f1 = {pack2(t1[0], t1[1]), pack2(t1[2], t1[3])};
      const u32x2 f2 = {pack2(t2[0], t2[1]), pack2(t2[2], t2[3])};
      *(u32x2*)(smem + mt * 1024 + ofs1) = f1;
      *(u32x2*)(smem + mt * 1024 + ofs2) = f2;
      kf[i] = odd ? f2 : f1;
    }
    if (lowrg) {
      const float* hpart = (const float*)(smem + K1_HPART);
      float2 sv;
      sv.x = (hpart[l15] + hpart[16 + l15]) * (1.f / D_);
      float var = (hpart[32 + l15] + hpart[48 + l15]) * (1.f / D_) - sv.x * sv.x;
      var = var < 0.f ? 0.f : var;
      sv.y = rsqrtf(var + EPS);
      const f32x4 v = (ex[0] - sv.x * cs1) * sv.y;
      *(u32x2*)(smem + ofs1) = u32x2{pack2(v[0], v[1]), pack2(v[2], v[3])};
    }
  }
  RAW_BARRIER();
  STAMP(0, 10);
  {
    const int par = l15 & 1;
    const int d0 = l15 - 4 * g - par;
    const bool vo0 = d0 >= 0, vo1 = d0 >= 2;
    const bool vp0 = d0 <= -2, vp1 = d0 <= 0;
    const float cexp = 0.25f * 1.4426950408889634f;
    const int vtr = ch * 512 + (4 * g + q4) * 32 + p4 * 8;
    unsigned od[2][2];
#pragma unroll
    for (int i = 0; i < 2; ++i) {
      const int mt = (i == 0 ? mtA : mtB);
      const u32x2 pk = *(const u32x2*)(xk + (mt - 1) * 1024 + ch * 512 + lane * 8);
      const bf16x8 va = lds_b(xv + (mt - 1) * 1024 + vtr, xv + mt * 1024 + vtr);
      const f32x4 z = {0, 0, 0, 0};
      const f32x4 sp = __builtin_amdgcn_mfma_f32_16x16x16bf16_1k(__builtin_bit_cast(s16x4, pk), __builtin_bit_cast(s16x4, qf[i]), z, 0, 0, 0);
      const f32x4 so = __builtin_amdgcn_mfma_f32_16x16x16bf16_1k(__builtin_bit_cast(s16x4, kf[i]), __builtin_bit_cast(s16x4, qf[i]), z, 0, 0, 0);
      const bool pvalid = !(panel == 0 && mt == 1);
      const float NEG = -3.0e38f;
      float l[4];
      l[0] = (vp0 && pvalid) ? (par ? sp[1] : sp[0]) : NEG;
      l[1] = (vp1 && pvalid) ? (par ? sp[3] : sp[2]) : NEG;
      l[2] = vo0 ? (par ? so[1] : so[0]) : NEG;
      l[3] = vo1 ? (par ? so[3] : so[2]) : NEG;
      float mx = __builtin_amdgcn_fmed3f(l[0], l[1], 3.4e38f);
      mx = __builtin_amdgcn_fmed3f(mx, l[2], 3.4e38f);
      mx = __builtin_amdgcn_fmed3f(mx, l[3], 3.4e38f);
      {
        auto r = __builtin_amdgcn_permlane32_swap(__float_as_uint(mx), __float_as_uint(mx), false, false);
        mx = __builtin_amdgcn_fmed3f(__uint_as_float(r[0]), __uint_as_float(r[1]), 3.4e38f);
        auto r2 = __builtin_amdgcn_permlane16_swap(__float_as_uint(mx), __float_as_uint(mx), false, false);
        mx = __builtin_amdgcn_fmed3f(__uint_as_float(r2[0]), __uint_as_float(r2[1]), 3.4e38f);
      }
      const float mc = mx * cexp;
      float p[4];
#pragma unroll
      for (int t = 0; t < 4; ++t) p[t] = __builtin_amdgcn_exp2f(fmaf(l[t], cexp, -mc));
      float sum = (p[0] + p[1]) + (p[2] + p[3]);
      {
        auto r = __builtin_amdgcn_permlane32_swap(__float_as_uint(sum), __float_as_uint(sum), false, false);
        sum = __uint_as_float(r[0]) + __uint_as_float(r[1]);
        auto r2 = __builtin_amdgcn_permlane16_swap(__float_as_uint(sum), __float_as_uint(sum), false, false);
        sum = __uint_as_float(r2[0]) + __uint_as_float(r2[1]);
      }
      u32x4 pb;
#pragma unroll
      for (int t = 0; t < 4; ++t) pb[t] = pack2(p[t], 0.f) << (16 * par);
      const f32x4 ot = mfma16(va, __builtin_bit_cast(bf16x8, pb), z);
      const float inv = __builtin_amdgcn_rcpf(sum);
      od[i][0] = pack2(ot[0] * inv, ot[1] * inv);
      od[i][1] = pack2(ot[2] * inv, ot[3] * inv);
    }
    swap16(od[0][0], od[1][0]);
    swap16(od[0][1], od[1][1]);
    const int mt = (g & 1) ? mtB : mtA;
    const int grow = panel * 128 + (mt - 1) * 16 + l15;
    *(u32x4*)(o + (size_t)grow * D_ + hp * 32 + ch * 16 + (g >> 1) * 8) = u32x4{od[0][0], od[0][1], od[1][0], od[1][1]};
  }
  asm volatile("" ::"v"(wm0), "v"(wm1), "v"(wm2));
  STAMP(0, 11);
  STAMPR(0, 15);
}

__global__ __launch_bounds__(512, 2) void k1_qkv_attn(const u16* __restrict__ x, const u16* __restrict__ wqkv,
                                                      u16* __restrict__ o, const u16* __restrict__ wo,
                                                      const u16* __restrict__ w1, const u16* __restrict__ w2,
                                                      const u16* __restrict__ outp) {
  extern __shared__ __attribute__((aligned(1024))) char smem[];
  k1_body(x, wqkv, o, wo, w1, w2, outp, smem);
}

static constexpr int G_STAGE = 16 * 1024;
static constexpr int G_R = 8;
static constexpr int G_LDS = G_R * G_STAGE;

template <int K, bool BIAS>
__device__ __forceinline__ void gemm64_body(const u16* __restrict__ A, const u16* __restrict__ W,
                                            const u16* __restrict__ res, const u16* __restrict__ bias,
                                            u16* __restrict__ out, float* __restrict__ stats, char* smem) {
  constexpr int NS = K / 64;
  constexpr int KID = BIAS ? 3 : 1;
  STAMPR(KID, 14);
  STAMP(KID, 0);
  const int tid = threadIdx.x, lane = tid & 63;
  const int w = __builtin_amdgcn_readfirstlane(tid >> 6);
  const int rg = w & 3, ch = w >> 2;
  const int bid = blockIdx.x;
  const int rt = (bid & 7) * 4 + (bid >> 6), ct = (bid >> 3) & 7;
  const int g = lane >> 4, l15 = lane & 15, q4 = l15 >> 2, p4 = lane & 3;

  const int row = rt * 64 + rg * 16 + l15;
  const int col8 = ct * 64 + ch * 32 + (g & 1) * 16 + (g >> 1) * 8;
  u32x4 rv = *(const u32x4*)(res + (size_t)row * D_ + col8);
  u32x4 bv = {0, 0, 0, 0};
  if (BIAS) bv = *(const u32x4*)(bias + col8);

  const char *srcA, *srcB;
  {
    const int rl = 8 * w + (lane >> 3);
    const int chunk = (lane & 7) ^ ((rl >> 1) & 7);
    srcA = (const char*)(A + (size_t)(rt * 64 + rl) * K + chunk * 8);
    const int b = 4 * w + g;
    const int c4 = b >> 1, pr = b & 1;
    const int chunkb = p4 ^ (2 * ((c4 >> 1) & 1));
    const int krow = 4 * c4 + q4;
    srcB = (const char*)(W + (size_t)krow * D_ + ct * 64 + pr * 32 + chunkb * 8);
  }
  auto issue = [&](int slot) {
    char* base = smem + slot * G_STAGE + w * 1024;
    glds16(srcA, base);
    glds16(srcB, base + 8192);
    srcA += 128;
    srcB += 64 * D_ * 2;
  };
  const int aoff0 = rg * 2048 + l15 * 128 + ((g ^ (l15 >> 1)) * 16);
  const int boff = 8192 + g * 1024 + ch * 256 + q4 * 64 + p4 * 8;
  const int sw = (g & 1) * 32;

  f32x4 acc[2] = {f32x4{0, 0, 0, 0}, f32x4{0, 0, 0, 0}};

#pragma unroll
  for (int s = 0; s < G_R - 1; ++s) issue(s);

  auto compute = [&](int slot) {
    const char* st = smem + slot * G_STAGE;
#pragma unroll
    for (int ks = 0; ks < 2; ++ks) {
      bf16x8 a = lds_a(st + (aoff0 ^ (ks * 64)));
#pragma unroll
      for (int j = 0; j < 2; ++j) {
        const int o2 = boff + ((j * 32) ^ sw);
        bf16x8 b = lds_b(st + o2 + (ks * 8 + 0) * 512, st + o2 + (ks * 8 + 1) * 512);
        acc[j] = mfma16(b, a, acc[j]);
      }
    }
  };

  STAMP(KID, 1);
  constexpr int NMAIN = NS - (G_R - 1);
  int slot = 0;
  for (int s = 0; s < NMAIN; ++s) {
    WAIT_V(2 * (G_R - 2));
    RAW_BARRIER();
    if (s == 0) STAMP(KID, 2);
    issue((slot + G_R - 1) & (G_R - 1));
    compute(slot);
    slot = (slot + 1) & (G_R - 1);
  }
#pragma unroll
  for (int t = 0; t < G_R - 1; ++t) {
    switch (G_R - 2 - t) {
      case 0: WAIT_V(0); break;
      case 1: WAIT_V(2); break;
      case 2: WAIT_V(4); break;
      case 3: WAIT_V(6); break;
      case 4: WAIT_V(8); break;
      case 5: WAIT_V(10); break;
      default: WAIT_V(12); break;
    }
    RAW_BARRIER();
    compute(slot);
    slot = (slot + 1) & (G_R - 1);
  }

  STAMP(KID, 3);
  {
    unsigned r0 = rv[0], r1 = rv[1], r2 = rv[2], r3 = rv[3];
    swap16(r0, r2);
    swap16(r1, r3);
    f32x4 v0 = acc[0], v1 = acc[1];
    v0[0] += bflo(r0);
    v0[1] += bfhi(r0);
    v0[2] += bflo(r1);
    v0[3] += bfhi(r1);
    v1[0] += bflo(r2);
    v1[1] += bfhi(r2);
    v1[2] += bflo(r3);
    v1[3] += bfhi(r3);
    if (BIAS) {
      unsigned b0 = bv[0], b1 = bv[1], b2 = bv[2], b3 = bv[3];
      swap16(b0, b2);
      swap16(b1, b3);
      v0[0] += bflo(b0);
      v0[1] += bfhi(b0);
      v0[2] += bflo(b1);
      v0[3] += bfhi(b1);
      v1[0] += bflo(b2);
      v1[1] += bfhi(b2);
      v1[2] += bflo(b3);
      v1[3] += bfhi(b3);
    }
    unsigned p0 = pack2(v0[0], v0[1]), p1 = pack2(v0[2], v0[3]);
    unsigned p2 = pack2(v1[0], v1[1]), p3 = pack2(v1[2], v1[3]);
    if (!BIAS) {
      float sm = dot2u(p0, 0x3F803F80u, 0.f), sq = dot2u(p0, p0, 0.f);
      sm = dot2u(p1, 0x3F803F80u, sm);
      sq = dot2u(p1, p1, sq);
      sm = dot2u(p2, 0x3F803F80u, sm);
      sq = dot2u(p2, p2, sq);
      sm = dot2u(p3, 0x3F803F80u, sm);
      sq = dot2u(p3, p3, sq);
      sm += __shfl_xor(sm, 16);
      sq += __shfl_xor(sq, 16);
      sm += __shfl_xor(sm, 32);
      sq += __shfl_xor(sq, 32);
      if (g == 0) *(float2*)(stats + (size_t)row * 32 + (ct * 2 + ch) * 2) = make_float2(sm, sq);
    }
    swap16(p0, p2);
    swap16(p1, p3);
    *(u32x4*)(out + (size_t)row * D_ + col8) = u32x4{p0, p1, p2, p3};
  }
  STAMP(KID, 4);
  STAMPR(KID, 15);
}

template <int K, bool BIAS>
__global__ __launch_bounds__(512, 2) void k_gemm64(const u16* __restrict__ A, const u16* __restrict__ W,
                                                   const u16* __restrict__ res, const u16* __restrict__ bias,
                                                   u16* __restrict__ out, float* __restrict__ stats) {
  extern __shared__ __attribute__((aligned(1024))) char smem[];
  gemm64_body<K, BIAS>(A, W, res, bias, out, stats, smem);
}

static constexpr int M1_RING = 65536;
static constexpr int M1_XCH = M1_RING + 5 * 16384;
static constexpr int K3_LDS = M1_XCH + 2048;

__device__ __forceinline__ float gelu_tanh(float v) {
  const float p = fmaf(v * v, -2.f * 1.4426950408889634f * 0.7978845608028654f * 0.044715f,
                       -2.f * 1.4426950408889634f * 0.7978845608028654f);
  const float e = __builtin_amdgcn_exp2f(v * p);
  return v * __builtin_amdgcn_rcpf(1.f + e);
}
__device__ __forceinline__ f32x4 ld16_async(const void* p) {
  f32x4 r;
  asm volatile("global_load_dwordx4 %0, %1, off" : "=v"(r) : "v"(p) : "memory");
  return r;
}
__device__ __forceinline__ u32x2 ld8_async(const void* p) {
  u32x2 r;
  asm volatile("global_load_dwordx2 %0, %1, off" : "=v"(r) : "v"(p) : "memory");
  return r;
}

__device__ __forceinline__ void mlp1_body(const u16* __restrict__ h, const u16* __restrict__ w1,
                                          const u16* __restrict__ b1, const float* __restrict__ stats,
                                          u16* __restrict__ h1, char* smem) {
  const int tid = threadIdx.x, lane = tid & 63;
  const int w = __builtin_amdgcn_readfirstlane(tid >> 6);
  const int cg = w & 3, rgp = w >> 2;
  const int bid = blockIdx.x;
  const int xcd = bid & 7, idx = bid >> 3;
  const int pan = (xcd >> 1) * 4 + (idx & 3), ct = (xcd & 1) * 7 + (idx >> 2);
  const int R0 = pan * 128, C0 = ct * 128;
  const int g = lane >> 4, l15 = lane & 15, q4 = l15 >> 2, p4 = lane & 3;
  STAMPR(2, 14);
  STAMP(2, 0);

  f32x4 sp[2];
#pragma unroll
  for (int i = 0; i < 2; ++i) sp[i] = ld16_async(stats + (size_t)(R0 + 16 * w + 8 * i + (lane >> 3)) * 32 + (lane & 7) * 4);
  u32x2 bv[2];
#pragma unroll
  for (int j = 0; j < 2; ++j) bv[j] = ld8_async(b1 + C0 + cg * 32 + j * 16 + 4 * g);

  const char* srcK[2];
  const char* srcA[2];
  const char* srcB[2];
#pragma unroll
  for (int r = 0; r < 2; ++r) {
    {
      const int rl = 8 * w + 4 * r + g;
      const int chunk = l15 ^ (rl & 15);
      srcK[r] = (const char*)(h + (size_t)(R0 + rl) * D_ + chunk * 8);
    }
    {
      const int rl = 2 * w + r;
      const int chunk = (lane & 48) | ((lane & 15) ^ rl);
      srcA[r] = (const char*)(h + (size_t)(R0 + 64 + rl) * D_ + chunk * 8);
    }
    const int b = 4 * (w + 8 * r) + g;
    const int c4 = b >> 2, pr = b & 3;
    const int chunkb = p4 ^ (2 * ((c4 >> 1) & 1));
    srcB[r] = (const char*)(w1 + (size_t)(4 * c4 + q4) * F_ + C0 + pr * 32 + chunkb * 8);
  }
  auto issueK = [&](char* dst) {
    glds16(srcK[0], dst + w * 2048);
    glds16(srcK[1], dst + w * 2048 + 1024);
    srcK[0] += 256;
    srcK[1] += 256;
  };
  auto issueA = [&](char* dst) {
    glds16(srcA[0], dst + w * 2048);
    glds16(srcA[1], dst + w * 2048 + 1024);
    srcA[0] += 16 * D_ * 2;
    srcA[1] += 16 * D_ * 2;
  };
  auto issueB = [&](char* dst) {
    glds16(srcB[0], dst + w * 1024);
    glds16(srcB[1], dst + w * 1024 + 8192);
    srcB[0] += 64 * F_ * 2;
    srcB[1] += 64 * F_ * 2;
  };
  char* ring = smem + M1_RING;
  issueK(smem);
  issueB(ring);
  issueB(ring + 16384);
  issueK(smem + 16384);

  int aoff[4], koff[4];
#pragma unroll
  for (int a = 0; a < 4; ++a) {
    aoff[a] = l15 * 1024 + ((g ^ l15 ^ (4 * a)) * 16);
    koff[a] = rgp * 4096 + l15 * 256 + ((g ^ l15 ^ (4 * a)) * 16);
  }
  const int boff = g * 2048 + cg * 256 + q4 * 64 + p4 * 8;
  const int sw = (g & 1) * 32;

  asm volatile("s_waitcnt vmcnt(8)" : "+v"(sp[0]), "+v"(sp[1]), "+v"(bv[0]), "+v"(bv[1]) : : "memory");
  float2* rstat = (float2*)(smem + M1_XCH + 1024);
#pragma unroll
  for (int i = 0; i < 2; ++i) {
    float sm = sp[i][0] + sp[i][2];
    float sq = sp[i][1] + sp[i][3];
#pragma unroll
    for (int m = 1; m < 8; m <<= 1) {
      sm += __shfl_xor(sm, m);
      sq += __shfl_xor(sq, m);
    }
    const float mean = sm * (1.f / D_);
    float var = sq * (1.f / D_) - mean * mean;
    var = var < 0.f ? 0.f : var;
    const float r1 = rsqrtf(var + EPS);
    const float v1 = var * r1 * r1;
    const float r2 = rsqrtf(v1 + EPS);
    const float rs = r1 * r2;
    if ((lane & 7) == 0) rstat[16 * w + 8 * i + (lane >> 3)] = make_float2(rs, -mean * rs);
  }

  bf16x8 bf[16][2];
  f32x4 acc[4][2], accC[2];
#pragma unroll
  for (int t = 0; t < 4; ++t) {
    acc[t][0] = f32x4{0, 0, 0, 0};
    acc[t][1] = f32x4{0, 0, 0, 0};
  }
  accC[0] = f32x4{0, 0, 0, 0};
  accC[1] = f32x4{0, 0, 0, 0};
  const bf16x8 ones = ones8();
  STAMP(2, 1);

#pragma unroll
  for (int s = 0; s < 8; ++s) {
    if (s & 1) WAIT_V(6); else WAIT_V(4);
    RAW_BARRIER();
    if (s == 0) STAMP(2, 2);
    const char* st = ring + (s % 5) * 16384;
#pragma unroll
    for (int ks = 0; ks < 2; ++ks) {
      const int kk = 2 * s + ks;
#pragma unroll
      for (int j = 0; j < 2; ++j) {
        const int o2 = boff + ((j * 32) ^ sw);
        bf[kk][j] = lds_b(st + o2 + (ks * 8 + 0) * 1024, st + o2 + (ks * 8 + 1) * 1024);
      }
      const char* ap = smem + (kk >> 2) * 16384 + koff[kk & 3];
      const bf16x8 a0 = lds_a(ap), a1 = lds_a(ap + 8192);
#pragma unroll
      for (int j = 0; j < 2; ++j) {
        acc[0][j] = mfma16(bf[kk][j], a0, acc[0][j]);
        acc[1][j] = mfma16(bf[kk][j], a1, acc[1][j]);
      }
    }
#pragma unroll
    for (int j = 0; j < 2; ++j) {
      const bf16x8 bs = rgp ? bf[2 * s + 1][j] : bf[2 * s][j];
      accC[j] = mfma16(bs, ones, accC[j]);
    }
    switch (s) {
      case 0: issueB(ring + 2 * 16384); issueB(ring + 3 * 16384); break;
      case 1: issueK(smem + 2 * 16384); break;
      case 2: issueB(ring + 4 * 16384); issueB(ring); break;
      case 3: issueK(smem + 3 * 16384); break;
      case 4: issueB(ring + 16384); issueB(ring + 2 * 16384); break;
      case 5: issueA(ring + 3 * 16384); break;
      case 6: issueA(ring + 4 * 16384); issueA(ring); break;
      default: issueA(ring + 16384); break;
    }
  }
  STAMP(2, 3);
  float* xch = (float*)(smem + M1_XCH);
  if (l15 == 0) {
    *(f32x4*)(xch + w * 32 + 4 * g) = accC[0];
    *(f32x4*)(xch + w * 32 + 16 + 4 * g) = accC[1];
  }
  WAIT_V(4);
  RAW_BARRIER();
  f32x4 tb[2];
  f32x4 cs[2];
#pragma unroll
  for (int j = 0; j < 2; ++j) {
    cs[j] = accC[j] + *(const f32x4*)(xch + (w ^ 4) * 32 + j * 16 + 4 * g);
    tb[j] = f32x4{bflo(bv[j][0]), bfhi(bv[j][0]), bflo(bv[j][1]), bfhi(bv[j][1])};
  }
  auto epi = [&](int t) {
    const float2 rm = rstat[(rgp + 2 * t) * 16 + l15];
    unsigned pk[2][2];
#pragma unroll
    for (int j = 0; j < 2; ++j) {
      float o4[4];
#pragma unroll
      for (int i = 0; i < 4; ++i) {
        const float tt = fmaf(rm.y, cs[j][i], tb[j][i]);
        o4[i] = gelu_tanh(fmaf(acc[t][j][i], rm.x, tt));
      }
      pk[j][0] = pack2(o4[0], o4[1]);
      pk[j][1] = pack2(o4[2], o4[3]);
    }
    swap16(pk[0][0], pk[1][0]);
    swap16(pk[0][1], pk[1][1]);
    const int row = R0 + (rgp + 2 * t) * 16 + l15;
    st16(h1, (unsigned)(row * F_ + C0 + cg * 32 + (g & 1) * 16 + (g >> 1) * 8) * 2u,
         u32x4{pk[0][0], pk[0][1], pk[1][0], pk[1][1]});
  };
  auto mm = [&](int t, const char* base) {
#pragma unroll
    for (int kk = 0; kk < 16; ++kk) {
      const bf16x8 a = lds_a(base + aoff[kk & 3] + (kk >> 2) * 256);
      acc[t][0] = mfma16(bf[kk][0], a, acc[t][0]);
      acc[t][1] = mfma16(bf[kk][1], a, acc[t][1]);
    }
  };
  if (rgp == 0) {
    epi(0);
    mm(2, ring + 3 * 16384);
    epi(1);
    WAIT_V(2);
    RAW_BARRIER();
    STAMP(2, 4);
    mm(3, ring + 0 * 16384);
    epi(2);
    epi(3);
  } else {
    mm(2, ring + 4 * 16384);
    epi(0);
    WAIT_V(1);
    RAW_BARRIER();
    epi(1);
    mm(3, ring + 1 * 16384);
    epi(2);
    epi(3);
  }
  STAMP(2, 5);
  STAMPR(2, 15);
}

__global__ __launch_bounds__(512, 2) void k3_mlp1(const u16* __restrict__ h, const u16* __restrict__ w1,
                                                  const u16* __restrict__ b1, const float* __restrict__ stats,
                                                  u16* __restrict__ h1) {
  extern __shared__ __attribute__((aligned(1024))) char smem[];
  mlp1_body(h, w1, b1, stats, h1, smem);
}

extern "C" void kernel_launch(void* const* d_in, const int* in_sizes, int n_in, void* d_out, int out_size, void* d_ws,
                              size_t ws_size, hipStream_t stream) {
  const u16* x = (const u16*)d_in[0];
  const u16* wqkv = (const u16*)d_in[1];
  const u16* wo = (const u16*)d_in[2];
  const u16* w1 = (const u16*)d_in[4];
  const u16* b1 = (const u16*)d_in[5];
  const u16* w2 = (const u16*)d_in[6];
  const u16* b2 = (const u16*)d_in[7];
  u16* out = (u16*)d_out;
  char* ws = (char*)d_ws;
  u16* o = (u16*)ws;
  u16* h = (u16*)(ws + (size_t)2 * 1024 * 1024);
  u16* h1 = (u16*)(ws + (size_t)4 * 1024 * 1024);
  float* stats = (float*)(ws + (size_t)12 * 1024 * 1024);

  static_assert(K1_LDS <= 160 * 1024 && G_LDS <= 160 * 1024 && K3_LDS <= 160 * 1024, "lds");
  (void)hipFuncSetAttribute((const void*)k1_qkv_attn, hipFuncAttributeMaxDynamicSharedMemorySize, K1_LDS);
  (void)hipFuncSetAttribute((const void*)k_gemm64<512, false>, hipFuncAttributeMaxDynamicSharedMemorySize, G_LDS);
  (void)hipFuncSetAttribute((const void*)k3_mlp1, hipFuncAttributeMaxDynamicSharedMemorySize, K3_LDS);
  (void)hipFuncSetAttribute((const void*)k_gemm64<1792, true>, hipFuncAttributeMaxDynamicSharedMemorySize, G_LDS);

  k1_qkv_attn<<<256, 512, K1_LDS, stream>>>(x, wqkv, o, wo, w1, w2, out);
  k_gemm64<512, false><<<256, 512, G_LDS, stream>>>(o, wo, x, b2, h, stats);
  k3_mlp1<<<224, 512, K3_LDS, stream>>>(h, w1, b1, stats, h1);
  k_gemm64<1792, true><<<256, 512, G_LDS, stream>>>(h1, w2, h, b2, out, stats);
}
```

```cpp
#include <hip/hip_runtime.h>
#include <stdint.h>

typedef __attribute__((ext_vector_type(4))) short s16x4;
typedef __attribute__((ext_vector_type(8))) short s16x8;
typedef __attribute__((ext_vector_type(2))) __bf16 bf16x2;
typedef __attribute__((ext_vector_type(8))) __bf16 bf16x8;
typedef __attribute__((ext_vector_type(4))) float f32x4;
typedef __attribute__((ext_vector_type(4))) unsigned u32x4;
typedef __attribute__((ext_vector_type(2))) unsigned u32x2;
typedef unsigned short u16;
#define LDS_AS __attribute__((address_space(3)))

#define WAIT_V(n) asm volatile("s_waitcnt vmcnt(%0)" ::"n"(n) : "memory")
#define WAIT_L0() asm volatile("s_waitcnt lgkmcnt(0)" ::: "memory")
#define RAW_BARRIER()                  \
  do {                                 \
    WAIT_L0();                         \
    __builtin_amdgcn_s_barrier();      \
    asm volatile("" ::: "memory");     \
  } while (0)

#define SCHED_PIN() __builtin_amdgcn_sched_barrier(0)
#define STAMP(kid, i)
#define STAMPR(kid, i)
static constexpr int S_ = 2048;
static constexpr int D_ = 512;
static constexpr int F_ = 1792;
static constexpr float EPS = 1e-5f;

__device__ __forceinline__ unsigned pack2(float a, float b) {
  bf16x2 v = {(__bf16)a, (__bf16)b};
  return __builtin_bit_cast(unsigned, v);
}
__device__ __forceinline__ float bflo(unsigned u) { return __uint_as_float(u << 16); }
__device__ __forceinline__ float bfhi(unsigned u) { return __uint_as_float(u & 0xffff0000u); }

__device__ __forceinline__ f32x4 mfma16(bf16x8 a, bf16x8 b, f32x4 c) {
  return __builtin_amdgcn_mfma_f32_16x16x32_bf16(a, b, c, 0, 0, 0);
}
__device__ __forceinline__ bf16x8 lds_a(const char* p) { return *(const bf16x8*)p; }
__device__ __forceinline__ bf16x8 lds_b(const char* p0, const char* p1) {
  s16x4 a = __builtin_amdgcn_ds_read_tr16_b64_v4i16((LDS_AS s16x4*)p0);
  s16x4 b = __builtin_amdgcn_ds_read_tr16_b64_v4i16((LDS_AS s16x4*)p1);
  s16x8 f = __builtin_shufflevector(a, b, 0, 1, 2, 3, 4, 5, 6, 7);
  return __builtin_bit_cast(bf16x8, f);
}
__device__ __forceinline__ float sumsq8(bf16x8 a, float c) {
  bf16x2 p0 = __builtin_shufflevector(a, a, 0, 1);
  bf16x2 p1 = __builtin_shufflevector(a, a, 2, 3);
  bf16x2 p2 = __builtin_shufflevector(a, a, 4, 5);
  bf16x2 p3 = __builtin_shufflevector(a, a, 6, 7);
  c = __builtin_amdgcn_fdot2_f32_bf16(p0, p0, c, false);
  c = __builtin_amdgcn_fdot2_f32_bf16(p1, p1, c, false);
  c = __builtin_amdgcn_fdot2_f32_bf16(p2, p2, c, false);
  c = __builtin_amdgcn_fdot2_f32_bf16(p3, p3, c, false);
  return c;
}
__device__ __forceinline__ float dot2u(unsigned a, unsigned b, float c) {
  return __builtin_amdgcn_fdot2_f32_bf16(__builtin_bit_cast(bf16x2, a), __builtin_bit_cast(bf16x2, b), c, false);
}
__device__ __forceinline__ float dot8(u32x4 a, u32x4 b) {
  const unsigned a0 = a[0], a1 = a[1], a2 = a[2], a3 = a[3];
  const unsigned b0 = b[0], b1 = b[1], b2 = b[2], b3 = b[3];
  float c = 0.f;
  c = dot2u(a0, b0, c);
  c = dot2u(a1, b1, c);
  c = dot2u(a2, b2, c);
  c = dot2u(a3, b3, c);
  return c;
}
__device__ __forceinline__ bf16x8 ones8() {
  s16x8 o = {0x3F80, 0x3F80, 0x3F80, 0x3F80, 0x3F80, 0x3F80, 0x3F80, 0x3F80};
  return __builtin_bit_cast(bf16x8, o);
}
__device__ __forceinline__ void glds16(const void* g, char* l) {
  __builtin_amdgcn_global_load_lds((const unsigned*)g, (unsigned*)l, 16, 0, 0);
}

#define ST_AUX 16
#define ST_AUX0 0
#define ST_AUX1 0
#define ST_AUX3 0
template <int AUX>
__device__ __forceinline__ void st16(void* base, unsigned off, u32x4 v) {
  __amdgpu_buffer_rsrc_t r = __builtin_amdgcn_make_buffer_rsrc(base, 0, 0x7fffffff, 0x00020000);
  __builtin_amdgcn_raw_buffer_store_b128(v, r, off, 0, AUX);
}
template <int AUX>
__device__ __forceinline__ void st8(void* base, unsigned off, u32x2 v) {
  __amdgpu_buffer_rsrc_t r = __builtin_amdgcn_make_buffer_rsrc(base, 0, 0x7fffffff, 0x00020000);
  __builtin_amdgcn_raw_buffer_store_b64(v, r, off, 0, AUX);
}
__device__ __forceinline__ void swap16(unsigned& a, unsigned& b) {
  auto r = __builtin_amdgcn_permlane16_swap(a, b, false, false);
  a = r[0];
  b = r[1];
}

static constexpr int K1_STAGE = 30 * 1024;
#define K1R 4
static constexpr int K1_R = K1R;
static constexpr int K1_ABYTES = 18 * 1024;
static constexpr int K1_STAT = K1_R * K1_STAGE;
static constexpr int K1_COLP = K1_STAT + 144 * 8;
static constexpr int K1_HPART = K1_COLP + 4 * 96 * 4;
static constexpr int K1_ONES = K1_HPART + 4 * 16 * 8;
static constexpr int K1_LDS = K1_ONES + 16;
static constexpr int QKV_PITCH = 208;

__device__ __forceinline__ void k1_body(const u16* __restrict__ x, const u16* __restrict__ wqkv, u16* __restrict__ o,
                                        const u16* __restrict__ wo, const u16* __restrict__ w1,
                                        const u16* __restrict__ w2, const u16* __restrict__ outp, char* smem) {
  const int tid = threadIdx.x, lane = tid & 63;
  const int w = __builtin_amdgcn_readfirstlane(tid >> 6);
  const int rg = w >> 1, ch = w & 1;
  const int bid = blockIdx.x;
  const int panel = ((bid & 7) >> 1) * 4 + ((bid >> 3) & 3), hp = (bid & 1) * 8 + (bid >> 5);
  const int row0 = panel * 128 - 16;
  STAMPR(0, 14);
  STAMP(0, 0);
  const int g = lane >> 4, l15 = lane & 15, q4 = l15 >> 2, p4 = lane & 3;

  const char* src[4];
  int stride[4];
#pragma unroll
  for (int i = 0; i < 4; ++i) {
    const int p = w + 8 * i;
    if (p < 18) {
      const int rl = 8 * p + (lane >> 3);
      const int chunk = (lane & 7) ^ ((rl >> 1) & 7);
      int gr = row0 + rl;
      gr = gr < 0 ? 0 : gr;
      src[i] = (const char*)(x + (size_t)gr * D_ + chunk * 8);
      stride[i] = 128;
    } else {
      const int pb = (p < 30 ? p : 29) - 18;
      const int b = 4 * pb + g;
      const int c4 = b / 3, pr = b - 3 * c4;
      const int chunk = p4 ^ (2 * ((c4 >> 1) & 1));
      const int krow = 4 * c4 + q4;
      src[i] = (const char*)(wqkv + (size_t)krow * (3 * D_) + pr * D_ + hp * 32 + chunk * 8);
      stride[i] = 64 * 3 * D_ * 2;
    }
  }
  const bool has4 = (w < 6);
#define K0ROT ((bid >> 5) & 7)
  int kp = K0ROT;
#pragma unroll
  for (int i = 0; i < 4; ++i) src[i] += kp * stride[i];
  auto issue = [&](int slot) {
    char* base = smem + slot * K1_STAGE + w * 1024;
    const int mul = (kp == 7) ? -7 : 1;
    kp = (kp + 1) & 7;
#pragma unroll
    for (int i = 0; i < 3; ++i) {
      glds16(src[i], base + i * 8192);
      src[i] += mul * stride[i];
    }
    if (has4) {
      glds16(src[3], base + 3 * 8192);
      src[3] += mul * stride[3];
    }
  };

  const int mtA = 1 + 2 * rg + ch, mtB = 2 + 2 * rg - ch;
  const int aoff0 = l15 * 128 + ((g ^ (l15 >> 1)) * 16);
  const int boff = K1_ABYTES + g * 1536 + q4 * 64 + ((ch ^ (g & 1)) * 32) + p4 * 8;

  f32x4 acc[2][3], accR = {0, 0, 0, 0}, accG = {0, 0, 0, 0}, ex[2] = {f32x4{0, 0, 0, 0}, f32x4{0, 0, 0, 0}};
#pragma unroll
  for (int j = 0; j < 3; ++j) {
    acc[0][j] = f32x4{0, 0, 0, 0};
    acc[1][j] = f32x4{0, 0, 0, 0};
  }
  const bf16x8 ones = ones8();
  const bool odd = (rg & 1) != 0;
  const bool lowrg = rg < 2;
  const int jj1 = odd ? 2 : 1, jj2 = odd ? 1 : 2;
  const int boff1 = boff + jj1 * 256, boff2 = boff + jj2 * 256;

  const int kx = rg & 1;
  const int bko[2] = {kx * 6144, (kx ^ 1) * 6144};
  const int ako[2] = {aoff0 ^ (kx * 64), aoff0 ^ (kx * 64) ^ 64};
  const int hko[2] = {lowrg ? ako[0] : K1_ONES, lowrg ? ako[1] : K1_ONES};
  const bool qh = lowrg && ch == 1;
  const int qko = qh ? ako[0] : K1_ONES;
  if (tid < 4) *(unsigned*)(smem + K1_ONES + tid * 4) = 0x3F803F80u;
  struct Frag {
    bf16x8 a0, a1, ah, b[3];
  };
  bf16x8 fq;
  auto rd = [&](Frag& f, int slot, int i) {
    const char* st = smem + slot * K1_STAGE;
    f.a0 = lds_a(st + mtA * 2048 + ako[i]);
    f.b[0] = lds_b(st + boff + bko[i], st + boff + bko[i] + 768);
    f.b[1] = lds_b(st + boff1 + bko[i], st + boff1 + bko[i] + 768);
    f.b[2] = lds_b(st + boff2 + bko[i], st + boff2 + bko[i] + 768);
    f.a1 = lds_a(st + mtB * 2048 + ako[i]);
    f.ah = lds_a(smem + (lowrg ? slot * K1_STAGE : 0) + hko[i]);
    if (i == 0) fq = lds_a(smem + (qh ? slot * K1_STAGE : 0) + qko);
  };
  auto mm = [&](const Frag& f, int i) {
#pragma unroll
    for (int j = 0; j < 3; ++j) acc[0][j] = mfma16(f.b[j], f.a0, acc[0][j]);
    accR = mfma16(ones, f.a0, accR);
    accG = mfma16(f.a0, f.a0, accG);
#pragma unroll
    for (int j = 0; j < 3; ++j) acc[1][j] = mfma16(f.b[j], f.a1, acc[1][j]);
    ex[0] = mfma16(f.b[1], f.ah, ex[0]);
    if (i == 0) {
      const bf16x8 pp = lowrg ? f.ah : f.b[0];
      ex[1] = mfma16(pp, fq, ex[1]);
    }
  };

#pragma unroll
  for (int s = 0; s < K1_R - 1; ++s) issue(s);

  Frag fx, fy;
#pragma unroll
  for (int s = 0; s < 8; ++s) {
    const int rem = 7 - s;
    if (rem >= K1_R - 2) {
      if (has4) WAIT_V(4 * (K1_R - 2)); else WAIT_V(3 * (K1_R - 2));
    } else if (rem == 2) {
      if (has4) WAIT_V(8); else WAIT_V(6);
    } else if (rem == 1) {
      if (has4) WAIT_V(4); else WAIT_V(3);
    } else {
      WAIT_V(0);
    }
    RAW_BARRIER();
    STAMP(0, 1 + s);
    rd(fx, s % K1_R, 0);
    SCHED_PIN();
    if (s > 0) mm(fy, 1);
    SCHED_PIN();
    asm volatile("" ::"v"(fx.a0), "v"(fx.a1), "v"(fx.ah), "v"(fx.b[0]), "v"(fx.b[1]), "v"(fx.b[2]), "v"(fq));
    rd(fy, s % K1_R, 1);
    SCHED_PIN();
    mm(fx, 0);
    SCHED_PIN();
    if (s + K1_R - 1 < 8) issue((s + K1_R - 1) % K1_R);
    SCHED_PIN();
    asm volatile("" ::"v"(fy.a0), "v"(fy.a1), "v"(fy.ah), "v"(fy.b[0]), "v"(fy.b[1]), "v"(fy.b[2]));
  }
  mm(fy, 1);

  STAMP(0, 9);
  u32x4 wm0, wm1, wm2;
  {
    const int t16 = (bid * 512 + tid) * 16;
    const char* p0 = (t16 < 1792 * 1024) ? (const char*)w1 + t16 : (const char*)wo + (t16 - 1792 * 1024) ;
    const char* p1 = (t16 < 1792 * 1024) ? (const char*)w2 + t16 : (const char*)wo + (t16 - 1792 * 1024) + 256 * 1024;
    wm0 = *(const u32x4*)p0;
    wm1 = *(const u32x4*)p1;
    const int j = (bid >> 3) * 8 + (tid & 7);
    const char* p2 = (j < 176) ? (const char*)o + (size_t)(2 * 1024 * 1024) + (size_t)j * 65536 : (const char*)outp + (size_t)(j - 176) * 16384;
    wm2 = u32x4{0, 0, 0, 0};
    if (tid < 8) wm2 = *(const u32x4*)p2;
  }
  {
    float2* stat = (float2*)(smem + K1_STAT);
    float* colp = (float*)(smem + K1_COLP);
    float* hpart = (float*)(smem + K1_HPART);
    const int e = l15 & 3;
    const bool diag = (g == (l15 >> 2));
    const float gq = e == 0 ? accG[0] : (e == 1 ? accG[1] : (e == 2 ? accG[2] : accG[3]));
    if (diag) {
      const float mean = accR[0] * (1.f / D_);
      float var = gq * (1.f / D_) - mean * mean;
      var = var < 0.f ? 0.f : var;
      stat[mtA * 16 + l15] = make_float2(mean, rsqrtf(var + EPS));
    }
    if (lowrg) {
      const float hq = e == 0 ? ex[1][0] : (e == 1 ? ex[1][1] : (e == 2 ? ex[1][2] : ex[1][3]));
      if (diag) hpart[ch * 32 + rg * 16 + l15] = hq;
    } else if (l15 == 0) {
      *(f32x4*)(colp + (rg - 2) * 32 + ch * 16 + 4 * g) = ex[1];
      *(f32x4*)(colp + rg * 32 + ch * 16 + 4 * g) = ex[0];
    }
  }
  RAW_BARRIER();
  u32x2 qf[2], kf[2];
  char* xk = smem;
  char* xv = smem + 9 * 1024;
  {
    const float2* stat = (const float2*)(smem + K1_STAT);
    const float* colp = (const float*)(smem + K1_COLP);
    const f32x4 cs0 = *(const f32x4*)(colp + ch * 16 + 4 * g) + *(const f32x4*)(colp + 32 + ch * 16 + 4 * g);
    const f32x4 cs1 = *(const f32x4*)(colp + (1 + jj1) * 32 + ch * 16 + 4 * g);
    const f32x4 cs2 = *(const f32x4*)(colp + (1 + jj2) * 32 + ch * 16 + 4 * g);
    const int kofs = ch * 512 + lane * 8, vofs = 9 * 1024 + ch * 512 + l15 * 32 + g * 8;
    const int ofs1 = odd ? vofs : kofs, ofs2 = odd ? kofs : vofs;
#pragma unroll
    for (int i = 0; i < 2; ++i) {
      const int mt = (i == 0 ? mtA : mtB);
      const float2 sv = stat[mt * 16 + l15];
      const f32x4 q = (acc[i][0] - sv.x * cs0) * sv.y;
      const f32x4 t1 = (acc[i][1] - sv.x * cs1) * sv.y;
      const f32x4 t2 = (acc[i][2] - sv.x * cs2) * sv.y;
      qf[i] = u32x2{pack2(q[0], q[1]), pack2(q[2], q[3])};
      const u32x2 f1 = {pack2(t1[0], t1[1]), pack2(t1[2], t1[3])};
      const u32x2 f2 = {pack2(t2[0], t2[1]), pack2(t2[2], t2[3])};
      *(u32x2*)(smem + mt * 1024 + ofs1) = f1;
      *(u32x2*)(smem + mt * 1024 + ofs2) = f2;
      kf[i] = odd ? f2 : f1;
    }
    if (lowrg) {
      const float* hpart = (const float*)(smem + K1_HPART);
      float2 sv;
      sv.x = (hpart[l15] + hpart[16 + l15]) * (1.f / D_);
      float var = (hpart[32 + l15] + hpart[48 + l15]) * (1.f / D_) - sv.x * sv.x;
      var = var < 0.f ? 0.f : var;
      sv.y = rsqrtf(var + EPS);
      const f32x4 v = (ex[0] - sv.x * cs1) * sv.y;
      *(u32x2*)(smem + ofs1) = u32x2{pack2(v[0], v[1]), pack2(v[2], v[3])};
    }
  }
  RAW_BARRIER();
  STAMP(0, 10);
  {
    const int par = l15 & 1;
    const int d0 = l15 - 4 * g - par;
    const bool vo0 = d0 >= 0, vo1 = d0 >= 2;
    const bool vp0 = d0 <= -2, vp1 = d0 <= 0;
    const float cexp = 0.25f * 1.4426950408889634f;
    const int vtr = ch * 512 + (4 * g + q4) * 32 + p4 * 8;
    unsigned od[2][2];
#pragma unroll
    for (int i = 0; i < 2; ++i) {
      const int mt = (i == 0 ? mtA : mtB);
      const u32x2 pk = *(const u32x2*)(xk + (mt - 1) * 1024 + ch * 512 + lane * 8);
      const bf16x8 va = lds_b(xv + (mt - 1) * 1024 + vtr, xv + mt * 1024 + vtr);
      const f32x4 z = {0, 0, 0, 0};
      f32x4 sp = __builtin_amdgcn_mfma_f32_16x16x16bf16_1k(__builtin_bit_cast(s16x4, pk), __builtin_bit_cast(s16x4, qf[i]), z, 0, 0, 0);
      const f32x4 so = __builtin_amdgcn_mfma_f32_16x16x16bf16_1k(__builtin_bit_cast(s16x4, kf[i]), __builtin_bit_cast(s16x4, qf[i]), z, 0, 0, 0);
      if (panel == 0 && mt == 1) sp = f32x4{-4e9f, -4e9f, -4e9f, -4e9f};
      const float NEG = -3.0e38f;
      float l[4];
      l[0] = vp0 ? (par ? sp[1] : sp[0]) : NEG;
      l[1] = vp1 ? (par ? sp[3] : sp[2]) : NEG;
      l[2] = vo0 ? (par ? so[1] : so[0]) : NEG;
      l[3] = vo1 ? (par ? so[3] : so[2]) : NEG;
      float mx = __builtin_amdgcn_fmed3f(l[0], l[1], 3.4e38f);
      mx = __builtin_amdgcn_fmed3f(mx, l[2], 3.4e38f);
      mx = __builtin_amdgcn_fmed3f(mx, l[3], 3.4e38f);
      {
        auto r = __builtin_amdgcn_permlane32_swap(__float_as_uint(mx), __float_as_uint(mx), false, false);
        mx = __builtin_amdgcn_fmed3f(__uint_as_float(r[0]), __uint_as_float(r[1]), 3.4e38f);
        auto r2 = __builtin_amdgcn_permlane16_swap(__float_as_uint(mx), __float_as_uint(mx), false, false);
        mx = __builtin_amdgcn_fmed3f(__uint_as_float(r2[0]), __uint_as_float(r2[1]), 3.4e38f);
      }
      const float mc = mx * cexp;
      float p[4];
#pragma unroll
      for (int t = 0; t < 4; ++t) p[t] = __builtin_amdgcn_exp2f(fmaf(l[t], cexp, -mc));
      float sum = (p[0] + p[1]) + (p[2] + p[3]);
      {
        auto r = __builtin_amdgcn_permlane32_swap(__float_as_uint(sum), __float_as_uint(sum), false, false);
        sum = __uint_as_float(r[0]) + __uint_as_float(r[1]);
        auto r2 = __builtin_amdgcn_permlane16_swap(__float_as_uint(sum), __float_as_uint(sum), false, false);
        sum = __uint_as_float(r2[0]) + __uint_as_float(r2[1]);
      }
      u32x4 pb;
#pragma unroll
      for (int t = 0; t < 4; ++t) pb[t] = pack2(p[t], 0.f) << (16 * par);
      const f32x4 ot = mfma16(va, __builtin_bit_cast(bf16x8, pb), z);
      const float inv = __builtin_amdgcn_rcpf(sum);
      od[i][0] = pack2(ot[0] * inv, ot[1] * inv);
      od[i][1] = pack2(ot[2] * inv, ot[3] * inv);
    }
    swap16(od[0][0], od[1][0]);
    swap16(od[0][1], od[1][1]);
    const int mt = (g & 1) ? mtB : mtA;
    const int grow = panel * 128 + (mt - 1) * 16 + l15;
    st16<ST_AUX0>(o, (unsigned)(grow * D_ + hp * 32 + ch * 16 + (g >> 1) * 8) * 2u, u32x4{od[0][0], od[0][1], od[1][0], od[1][1]});
  }
  asm volatile("" ::"v"(wm0), "v"(wm1), "v"(wm2));
  STAMP(0, 11);
  STAMPR(0, 15);
}

__global__ __launch_bounds__(512, 2) void k1_qkv_attn(const u16* __restrict__ x, const u16* __restrict__ wqkv,
                                                      u16* __restrict__ o, const u16* __restrict__ wo,
                                                      const u16* __restrict__ w1, const u16* __restrict__ w2,
                                                      const u16* __restrict__ outp) {
  extern __shared__ __attribute__((aligned(1024))) char smem[];
  k1_body(x, wqkv, o, wo, w1, w2, outp, smem);
}

static constexpr int G_STAGE = 16 * 1024;
static constexpr int G_R = 8;
static constexpr int G_LDS = G_R * G_STAGE;

template <int K, bool BIAS>
__device__ __forceinline__ void gemm64_body(const u16* __restrict__ A, const u16* __restrict__ W,
                                            const u16* __restrict__ res, const u16* __restrict__ bias,
                                            u16* __restrict__ out, float* __restrict__ stats, char* smem) {
  constexpr int NS = K / 64;
  constexpr int KID = BIAS ? 3 : 1;
  STAMPR(KID, 14);
  STAMP(KID, 0);
  const int tid = threadIdx.x, lane = tid & 63;
  const int w = __builtin_amdgcn_readfirstlane(tid >> 6);
  const int rg = w & 3, ch = w >> 2;
  const int bid = blockIdx.x;
  const int rt = (bid & 7) * 4 + (bid >> 6), ct = (bid >> 3) & 7;
  const int g = lane >> 4, l15 = lane & 15, q4 = l15 >> 2, p4 = lane & 3;

  const int row = rt * 64 + rg * 16 + l15;
  const int col8 = ct * 64 + ch * 32 + (g & 1) * 16 + (g >> 1) * 8;
  u32x4 rv = *(const u32x4*)(res + (size_t)row * D_ + col8);
  u32x4 bv = {0, 0, 0, 0};
  if (BIAS) bv = *(const u32x4*)(bias + col8);

  const char *srcA, *srcB;
  {
    const int rl = 8 * w + (lane >> 3);
    const int chunk = (lane & 7) ^ ((rl >> 1) & 7);
    srcA = (const char*)(A + (size_t)(rt * 64 + rl) * K + chunk * 8);
    const int b = 4 * w + g;
    const int c4 = b >> 1, pr = b & 1;
    const int chunkb = p4 ^ (2 * ((c4 >> 1) & 1));
    const int krow = 4 * c4 + q4;
    srcB = (const char*)(W + (size_t)krow * D_ + ct * 64 + pr * 32 + chunkb * 8);
  }
  auto issue = [&](int slot) {
    char* base = smem + slot * G_STAGE + w * 1024;
    glds16(srcA, base);
    glds16(srcB, base + 8192);
    srcA += 128;
    srcB += 64 * D_ * 2;
  };
  const int aoff0 = rg * 2048 + l15 * 128 + ((g ^ (l15 >> 1)) * 16);
  const int boff = 8192 + g * 1024 + ch * 256 + q4 * 64 + p4 * 8;
  const int sw = (g & 1) * 32;

  f32x4 acc[2] = {f32x4{0, 0, 0, 0}, f32x4{0, 0, 0, 0}};

#pragma unroll
  for (int s = 0; s < G_R - 1; ++s) issue(s);

  auto compute = [&](int slot) {
    const char* st = smem + slot * G_STAGE;
#pragma unroll
    for (int ks = 0; ks < 2; ++ks) {
      bf16x8 a = lds_a(st + (aoff0 ^ (ks * 64)));
#pragma unroll
      for (int j = 0; j < 2; ++j) {
        const int o2 = boff + ((j * 32) ^ sw);
        bf16x8 b = lds_b(st + o2 + (ks * 8 + 0) * 512, st + o2 + (ks * 8 + 1) * 512);
        acc[j] = mfma16(b, a, acc[j]);
      }
    }
  };

  STAMP(KID, 1);
  constexpr int NMAIN = NS - (G_R - 1);
  int slot = 0;
  for (int s = 0; s < NMAIN; ++s) {
    WAIT_V(2 * (G_R - 2));
    RAW_BARRIER();
    if (s == 0) STAMP(KID, 2);
    issue((slot + G_R - 1) & (G_R - 1));
    compute(slot);
    slot = (slot + 1) & (G_R - 1);
  }
#pragma unroll
  for (int t = 0; t < G_R - 1; ++t) {
    switch (G_R - 2 - t) {
      case 0: WAIT_V(0); break;
      case 1: WAIT_V(2); break;
      case 2: WAIT_V(4); break;
      case 3: WAIT_V(6); break;
      case 4: WAIT_V(8); break;
      case 5: WAIT_V(10); break;
      default: WAIT_V(12); break;
    }
    RAW_BARRIER();
    compute(slot);
    slot = (slot + 1) & (G_R - 1);
  }

  STAMP(KID, 3);
  {
    unsigned r0 = rv[0], r1 = rv[1], r2 = rv[2], r3 = rv[3];
    swap16(r0, r2);
    swap16(r1, r3);
    f32x4 v0 = acc[0], v1 = acc[1];
    v0[0] += bflo(r0);
    v0[1] += bfhi(r0);
    v0[2] += bflo(r1);
    v0[3] += bfhi(r1);
    v1[0] += bflo(r2);
    v1[1] += bfhi(r2);
    v1[2] += bflo(r3);
    v1[3] += bfhi(r3);
    if (BIAS) {
      unsigned b0 = bv[0], b1 = bv[1], b2 = bv[2], b3 = bv[3];
      swap16(b0, b2);
      swap16(b1, b3);
      v0[0] += bflo(b0);
      v0[1] += bfhi(b0);
      v0[2] += bflo(b1);
      v0[3] += bfhi(b1);
      v1[0] += bflo(b2);
      v1[1] += bfhi(b2);
      v1[2] += bflo(b3);
      v1[3] += bfhi(b3);
    }
    unsigned p0 = pack2(v0[0], v0[1]), p1 = pack2(v0[2], v0[3]);
    unsigned p2 = pack2(v1[0], v1[1]), p3 = pack2(v1[2], v1[3]);
    if (!BIAS) {
      float sm = dot2u(p0, 0x3F803F80u, 0.f), sq = dot2u(p0, p0, 0.f);
      sm = dot2u(p1, 0x3F803F80u, sm);
      sq = dot2u(p1, p1, sq);
      sm = dot2u(p2, 0x3F803F80u, sm);
      sq = dot2u(p2, p2, sq);
      sm = dot2u(p3, 0x3F803F80u, sm);
      sq = dot2u(p3, p3, sq);
      sm += __shfl_xor(sm, 16);
      sq += __shfl_xor(sq, 16);
      sm += __shfl_xor(sm, 32);
      sq += __shfl_xor(sq, 32);
      if (g == 0) st8<ST_AUX1>(stats, (unsigned)(row * 32 + (ct * 2 + ch) * 2) * 4u, u32x2{__float_as_uint(sm), __float_as_uint(sq)});
    }
    swap16(p0, p2);
    swap16(p1, p3);
    st16<(BIAS ? ST_AUX3 : ST_AUX1)>(out, (unsigned)(row * D_ + col8) * 2u, u32x4{p0, p1, p2, p3});
  }
  STAMP(KID, 4);
  STAMPR(KID, 15);
}

template <int K, bool BIAS>
__global__ __launch_bounds__(512, 2) void k_gemm64(const u16* __restrict__ A, const u16* __restrict__ W,
                                                   const u16* __restrict__ res, const u16* __restrict__ bias,
                                                   u16* __restrict__ out, float* __restrict__ stats) {
  extern __shared__ __attribute__((aligned(1024))) char smem[];
  gemm64_body<K, BIAS>(A, W, res, bias, out, stats, smem);
}

static constexpr int M1_RING = 65536;
static constexpr int M1_XCH = M1_RING + 5 * 16384;
static constexpr int K3_LDS = M1_XCH + 2048;

__device__ __forceinline__ float gelu_tanh(float v) {
  const float p = fmaf(v * v, -2.f * 1.4426950408889634f * 0.7978845608028654f * 0.044715f,
                       -2.f * 1.4426950408889634f * 0.7978845608028654f);
  const float e = __builtin_amdgcn_exp2f(v * p);
  return v * __builtin_amdgcn_rcpf(1.f + e);
}
__device__ __forceinline__ f32x4 ld16_async(const void* p) {
  f32x4 r;
  asm volatile("global_load_dwordx4 %0, %1, off" : "=v"(r) : "v"(p) : "memory");
  return r;
}
__device__ __forceinline__ u32x2 ld8_async(const void* p) {
  u32x2 r;
  asm volatile("global_load_dwordx2 %0, %1, off" : "=v"(r) : "v"(p) : "memory");
  return r;
}

__device__ __forceinline__ void mlp1_body(const u16* __restrict__ h, const u16* __restrict__ w1,
                                          const u16* __restrict__ b1, const float* __restrict__ stats,
                                          u16* __restrict__ h1, char* smem) {
  const int tid = threadIdx.x, lane = tid & 63;
  const int w = __builtin_amdgcn_readfirstlane(tid >> 6);
  const int cg = w & 3, rgp = w >> 2;
  const int bid = blockIdx.x;
  const int xcd = bid & 7, idx = bid >> 3;
  const int pan = (xcd >> 1) * 4 + (idx & 3), ct = (xcd & 1) * 7 + (idx >> 2);
  const int R0 = pan * 128, C0 = ct * 128;
  const int g = lane >> 4, l15 = lane & 15, q4 = l15 >> 2, p4 = lane & 3;
  STAMPR(2, 14);
  STAMP(2, 0);

  f32x4 sp[2];
#pragma unroll
  for (int i = 0; i < 2; ++i) sp[i] = ld16_async(stats + (size_t)(R0 + 16 * w + 8 * i + (lane >> 3)) * 32 + (lane & 7) * 4);
  u32x2 bv[2];
#pragma unroll
  for (int j = 0; j < 2; ++j) bv[j] = ld8_async(b1 + C0 + cg * 32 + j * 16 + 4 * g);

  const char* srcK[2];
  const char* srcA[2];
  const char* srcB[2];
#pragma unroll
  for (int r = 0; r < 2; ++r) {
    {
      const int rl = 8 * w + 4 * r + g;
      const int chunk = l15 ^ (rl & 15);
      srcK[r] = (const char*)(h + (size_t)(R0 + rl) * D_ + chunk * 8);
    }
    {
      const int rl = 2 * w + r;
      const int chunk = (lane & 48) | ((lane & 15) ^ rl);
      srcA[r] = (const char*)(h + (size_t)(R0 + 64 + rl) * D_ + chunk * 8);
    }
    const int b = 4 * (w + 8 * r) + g;
    const int c4 = b >> 2, pr = b & 3;
    const int chunkb = p4 ^ (2 * ((c4 >> 1) & 1));
    srcB[r] = (const char*)(w1 + (size_t)(4 * c4 + q4) * F_ + C0 + pr * 32 + chunkb * 8);
  }
  auto issueK = [&](char* dst) {
    glds16(srcK[0], dst + w * 2048);
    glds16(srcK[1], dst + w * 2048 + 1024);
    srcK[0] += 256;
    srcK[1] += 256;
  };
  auto issueA = [&](char* dst) {
    glds16(srcA[0], dst + w * 2048);
    glds16(srcA[1], dst + w * 2048 + 1024);
    srcA[0] += 16 * D_ * 2;
    srcA[1] += 16 * D_ * 2;
  };
  auto issueB = [&](char* dst) {
    glds16(srcB[0], dst + w * 1024);
    glds16(srcB[1], dst + w * 1024 + 8192);
    srcB[0] += 64 * F_ * 2;
    srcB[1] += 64 * F_ * 2;
  };
  char* ring = smem + M1_RING;
  issueK(smem);
  issueB(ring);
  issueB(ring + 16384);
  issueK(smem + 16384);

  int aoff[4], koff[4];
#pragma unroll
  for (int a = 0; a < 4; ++a) {
    aoff[a] = l15 * 1024 + ((g ^ l15 ^ (4 * a)) * 16);
    koff[a] = rgp * 4096 + l15 * 256 + ((g ^ l15 ^ (4 * a)) * 16);
  }
  const int boff = g * 2048 + cg * 256 + q4 * 64 + p4 * 8;
  const int sw = (g & 1) * 32;

  asm volatile("s_waitcnt vmcnt(8)" : "+v"(sp[0]), "+v"(sp[1]), "+v"(bv[0]), "+v"(bv[1]) : : "memory");
  float2* rstat = (float2*)(smem + M1_XCH + 1024);
#pragma unroll
  for (int i = 0; i < 2; ++i) {
    float sm = sp[i][0] + sp[i][2];
    float sq = sp[i][1] + sp[i][3];
#pragma unroll
    for (int m = 1; m < 8; m <<= 1) {
      sm += __shfl_xor(sm, m);
      sq += __shfl_xor(sq, m);
    }
    const float mean = sm * (1.f / D_);
    float var = sq * (1.f / D_) - mean * mean;
    var = var < 0.f ? 0.f : var;
    const float r1 = rsqrtf(var + EPS);
    const float v1 = var * r1 * r1;
    const float r2 = rsqrtf(v1 + EPS);
    const float rs = r1 * r2;
    if ((lane & 7) == 0) rstat[16 * w + 8 * i + (lane >> 3)] = make_float2(rs, -mean * rs);
  }

  bf16x8 bf[16][2];
  f32x4 acc[4][2], accC[2];
#pragma unroll
  for (int t = 0; t < 4; ++t) {
    acc[t][0] = f32x4{0, 0, 0, 0};
    acc[t][1] = f32x4{0, 0, 0, 0};
  }
  accC[0] = f32x4{0, 0, 0, 0};
  accC[1] = f32x4{0, 0, 0, 0};
  const bf16x8 ones = ones8();
  STAMP(2, 1);

#pragma unroll
  for (int s = 0; s < 8; ++s) {
    if (s & 1) WAIT_V(6); else WAIT_V(4);
    RAW_BARRIER();
    if (s == 0) STAMP(2, 2);
    const char* st = ring + (s % 5) * 16384;
#pragma unroll
    for (int ks = 0; ks < 2; ++ks) {
      const int kk = 2 * s + ks;
#pragma unroll
      for (int j = 0; j < 2; ++j) {
        const int o2 = boff + ((j * 32) ^ sw);
        bf[kk][j] = lds_b(st + o2 + (ks * 8 + 0) * 1024, st + o2 + (ks * 8 + 1) * 1024);
      }
      const char* ap = smem + (kk >> 2) * 16384 + koff[kk & 3];
      const bf16x8 a0 = lds_a(ap), a1 = lds_a(ap + 8192);
#pragma unroll
      for (int j = 0; j < 2; ++j) {
        acc[0][j] = mfma16(bf[kk][j], a0, acc[0][j]);
        acc[1][j] = mfma16(bf[kk][j], a1, acc[1][j]);
      }
    }
#pragma unroll
    for (int j = 0; j < 2; ++j) {
      const bf16x8 bs = rgp ? bf[2 * s + 1][j] : bf[2 * s][j];
      accC[j] = mfma16(bs, ones, accC[j]);
    }
    switch (s) {
      case 0: issueB(ring + 2 * 16384); issueB(ring + 3 * 16384); break;
      case 1: issueK(smem + 2 * 16384); break;
      case 2: issueB(ring + 4 * 16384); issueB(ring); break;
      case 3: issueK(smem + 3 * 16384); break;
      case 4: issueB(ring + 16384); issueB(ring + 2 * 16384); break;
      case 5: issueA(ring + 3 * 16384); break;
      case 6: issueA(ring + 4 * 16384); issueA(ring); break;
      default: issueA(ring + 16384); break;
    }
  }
  STAMP(2, 3);
  float* xch = (float*)(smem + M1_XCH);
  if (l15 == 0) {
    *(f32x4*)(xch + w * 32 + 4 * g) = accC[0];
    *(f32x4*)(xch + w * 32 + 16 + 4 * g) = accC[1];
  }
  WAIT_V(4);
  RAW_BARRIER();
  f32x4 tb[2];
  f32x4 cs[2];
#pragma unroll
  for (int j = 0; j < 2; ++j) {
    cs[j] = accC[j] + *(const f32x4*)(xch + (w ^ 4) * 32 + j * 16 + 4 * g);
    tb[j] = f32x4{bflo(bv[j][0]), bfhi(bv[j][0]), bflo(bv[j][1]), bfhi(bv[j][1])};
  }
  auto epi = [&](int t) {
    const float2 rm = rstat[(rgp + 2 * t) * 16 + l15];
    unsigned pk[2][2];
#pragma unroll
    for (int j = 0; j < 2; ++j) {
      float o4[4];
#pragma unroll
      for (int i = 0; i < 4; ++i) {
        const float tt = fmaf(rm.y, cs[j][i], tb[j][i]);
        o4[i] = gelu_tanh(fmaf(acc[t][j][i], rm.x, tt));
      }
      pk[j][0] = pack2(o4[0], o4[1]);
      pk[j][1] = pack2(o4[2], o4[3]);
    }
    swap16(pk[0][0], pk[1][0]);
    swap16(pk[0][1], pk[1][1]);
    const int row = R0 + (rgp + 2 * t) * 16 + l15;
    st16<ST_AUX>(h1, (unsigned)(row * F_ + C0 + cg * 32 + (g & 1) * 16 + (g >> 1) * 8) * 2u,
         u32x4{pk[0][0], pk[0][1], pk[1][0], pk[1][1]});
  };
  auto mm = [&](int t, const char* base) {
#pragma unroll
    for (int kk = 0; kk < 16; ++kk) {
      const bf16x8 a = lds_a(base + aoff[kk & 3] + (kk >> 2) * 256);
      acc[t][0] = mfma16(bf[kk][0], a, acc[t][0]);
      acc[t][1] = mfma16(bf[kk][1], a, acc[t][1]);
    }
  };
  if (rgp == 0) {
    epi(0);
    mm(2, ring + 3 * 16384);
    epi(1);
    WAIT_V(2);
    RAW_BARRIER();
    STAMP(2, 4);
    mm(3, ring + 0 * 16384);
    epi(2);
    epi(3);
  } else {
    mm(2, ring + 4 * 16384);
    epi(0);
    WAIT_V(1);
    RAW_BARRIER();
    epi(1);
    mm(3, ring + 1 * 16384);
    epi(2);
    epi(3);
  }
  STAMP(2, 5);
  STAMPR(2, 15);
}

__global__ __launch_bounds__(512, 2) void k3_mlp1(const u16* __restrict__ h, const u16* __restrict__ w1,
                                                  const u16* __restrict__ b1, const float* __restrict__ stats,
                                                  u16* __restrict__ h1) {
  extern __shared__ __attribute__((aligned(1024))) char smem[];
  mlp1_body(h, w1, b1, stats, h1, smem);
}

extern "C" void kernel_launch(void* const* d_in, const int* in_sizes, int n_in, void* d_out, int out_size, void* d_ws,
                              size_t ws_size, hipStream_t stream) {
  const u16* x = (const u16*)d_in[0];
  const u16* wqkv = (const u16*)d_in[1];
  const u16* wo = (const u16*)d_in[2];
  const u16* w1 = (const u16*)d_in[4];
  const u16* b1 = (const u16*)d_in[5];
  const u16* w2 = (const u16*)d_in[6];
  const u16* b2 = (const u16*)d_in[7];
  u16* out = (u16*)d_out;
  char* ws = (char*)d_ws;
  u16* o = (u16*)ws;
  u16* h = (u16*)(ws + (size_t)2 * 1024 * 1024);
  u16* h1 = (u16*)(ws + (size_t)4 * 1024 * 1024);
  float* stats = (float*)(ws + (size_t)12 * 1024 * 1024);

  static_assert(K1_LDS <= 160 * 1024 && G_LDS <= 160 * 1024 && K3_LDS <= 160 * 1024, "lds");
  (void)hipFuncSetAttribute((const void*)k1_qkv_attn, hipFuncAttributeMaxDynamicSharedMemorySize, K1_LDS);
  (void)hipFuncSetAttribute((const void*)k_gemm64<512, false>, hipFuncAttributeMaxDynamicSharedMemorySize, G_LDS);
  (void)hipFuncSetAttribute((const void*)k3_mlp1, hipFuncAttributeMaxDynamicSharedMemorySize, K3_LDS);
  (void)hipFuncSetAttribute((const void*)k_gemm64<1792, true>, hipFuncAttributeMaxDynamicSharedMemorySize, G_LDS);

  k1_qkv_attn<<<256, 512, K1_LDS, stream>>>(x, wqkv, o, wo, w1, w2, out);
  k_gemm64<512, false><<<256, 512, G_LDS, stream>>>(o, wo, x, b2, h, stats);
  k3_mlp1<<<224, 512, K3_LDS, stream>>>(h, w1, b1, stats, h1);
  k_gemm64<1792, true><<<256, 512, G_LDS, stream>>>(h1, w2, h, b2, out, stats);
}
```
